```python
import torch

def _softmax(x, axis=-1):
    return torch.softmax(x, dim=axis)
def _gelu(x):
    return torch.nn.functional.gelu(x, approximate="tanh")
def _silu(x):
    return torch.nn.functional.silu(x)
def _sigmoid(x):
    return torch.sigmoid(x)
def _layernorm(x, axis=-1, eps=1e-5):
    m = x.mean(dim=axis, keepdim=True)
    v = x.var(dim=axis, keepdim=True, unbiased=False)
    return (x - m) / torch.sqrt(v + eps)
def _scatter_add(tgt, idx, src, axis=0):
    return tgt.clone().index_add_(axis, idx, src)
def _segment_sum(src, seg, n):
    out = torch.zeros((n,) + src.shape[1:], dtype=src.dtype)
    return out.index_add_(0, seg, src)
def _pad(x, pw):
    flat = tuple(v for pair in reversed(pw) for v in pair)
    return torch.nn.functional.pad(x, flat)

kernel_name = 'interleave_layers__linear_attn_block__glu_gated_bilinear__prompt'

def setup_inputs():

    _w = (lambda a: (lambda t: t.to(torch.bfloat16) if t.is_floating_point() else t)(torch.as_tensor(a)))
    def _A_setup():
        B, S, D = 1, 2048, 640
        return {
            "x": _w((lambda *s: torch.randn(*s, dtype=torch.bfloat16) * 0.05)(B, S, D)),
            "wqkv": _w((lambda *s: torch.randn(*s, dtype=torch.bfloat16) * 0.05)(D, 3 * D)),
            "wo": _w((lambda *s: torch.randn(*s, dtype=torch.bfloat16) * 0.05)(D, D)),
        }
    _A_in = _A_setup()

    def _B_setup():
        B, S, D, F = 1, 2048, 640, 1728
        return {
            "x": _w((lambda *s: torch.randn(*s, dtype=torch.bfloat16) * 0.05)(B, S, D)),
            "w_gate": _w((lambda *s: torch.randn(*s, dtype=torch.bfloat16) * 0.8)(D, F) * (D ** -0.5)),
            "w_up": _w((lambda *s: torch.randn(*s, dtype=torch.bfloat16) * 0.8)(D, F) * (D ** -0.5)),
            "w_down": _w((lambda *s: torch.randn(*s, dtype=torch.bfloat16) * 0.8)(F, D) * (F ** -0.5)),
        }
    _B_in = _B_setup()

    return {
        "A_x": _A_in["x"],
        "A_wqkv": _A_in["wqkv"],
        "A_wo": _A_in["wo"],
        "B_x": _B_in["x"],
        "B_w_gate": _B_in["w_gate"],
        "B_w_up": _B_in["w_up"],
        "B_w_down": _B_in["w_down"],
    }

def reference(A_x, A_wqkv, A_wo, B_x, B_w_gate, B_w_up, B_w_down):
    def _A_ref(x, wqkv, wo):
        B, S, D = x.shape
        H = 32; Dh = D // H
        qkv = (lambda x, *d: x.permute(*d))((x @ wqkv).reshape(B, S, 3, H, Dh), 2, 0, 3, 1, 4)
        q, k, v = qkv[0], qkv[1], qkv[2]
        phi = lambda t: torch.where(t > 0, t + 1, torch.exp(t)) + 1e-6
        pq, pk = phi(q), phi(k)
        kv = torch.einsum("bhsd,bhse->bhde", pk, v)
        z = torch.einsum("bhsd->bhd", pk)
        num = torch.einsum("bhqd,bhde->bhqe", pq, kv)
        den = torch.einsum("bhqd,bhd->bhq", pq, z)[..., None] + 1e-6
        o = num / den
        return (lambda x, *d: x.permute(*d))(o, 0, 2, 1, 3).reshape(B, S, D) @ wo

    def _B_ref(x, w_gate, w_up, w_down):
        g = torch.einsum("bsd,df->bsf", x, w_gate)
        u = torch.einsum("bsd,df->bsf", x, w_up)
        h = g * u
        return torch.einsum("bsf,fd->bsd", h, w_down)

    x = A_x
    x = x + _A_ref(_layernorm(x, axis=-1), A_wqkv, A_wo)
    x = x + _B_ref(_layernorm(x, axis=-1), B_w_gate, B_w_up, B_w_down)
    x = x + _A_ref(_layernorm(x, axis=-1), A_wqkv, A_wo)
    x = x + _B_ref(_layernorm(x, axis=-1), B_w_gate, B_w_up, B_w_down)
    return x
```

```cpp
#include <hip/hip_runtime.h>
#include <hip/hip_bf16.h>
#include <type_traits>

#define LAS __attribute__((address_space(3)))
#define GAS __attribute__((address_space(1)))
#define DI __device__ __forceinline__
typedef short bf16x8 __attribute__((ext_vector_type(8)));
typedef short s16x4 __attribute__((ext_vector_type(4)));
typedef float f32x4 __attribute__((ext_vector_type(4)));
typedef float f32x2 __attribute__((ext_vector_type(2)));
typedef unsigned u32x4 __attribute__((ext_vector_type(4)));
typedef unsigned u32x2 __attribute__((ext_vector_type(2)));
typedef GAS unsigned gu32;

constexpr int SEQ = 2048, DM = 640, FF = 1728;
constexpr int KSD = 20, KSF = 54, NMT = 128;
constexpr int FRAG = 1024;

constexpr size_t WS_CTL = 0;
constexpr size_t CTL_BYTES = 16384;
constexpr int CTL_TMO = 0;
constexpr int CTL_KV = 64;
constexpr size_t WS_WQKV = CTL_BYTES;
constexpr size_t WS_WO = WS_WQKV + (size_t)16 * 160 * FRAG;
constexpr size_t WS_WGU = WS_WO + (size_t)8 * 100 * FRAG;
constexpr size_t WS_WD = WS_WGU + (size_t)16 * 280 * FRAG;
constexpr size_t WS_CSQ = WS_WD + (size_t)8 * 272 * FRAG;
constexpr size_t WS_CSG = WS_CSQ + (size_t)16 * 20 * 128 * 4;
constexpr size_t XT_BYTES = (size_t)NMT * KSD * FRAG;
constexpr size_t WS_X = WS_CSG + (size_t)16 * 20 * 224 * 4;
constexpr size_t ST_BYTES = (size_t)SEQ * 64;
constexpr size_t WS_ST = WS_X + 4 * XT_BYTES;
constexpr size_t WS_O = WS_ST + 4 * ST_BYTES;
constexpr size_t HT_BYTES = (size_t)NMT * KSF * FRAG;
constexpr size_t WS_H = WS_O + 2 * XT_BYTES;
constexpr size_t KVP_SLOT = 3584;
constexpr size_t WS_KVP = WS_H + 2 * HT_BYTES;
constexpr size_t WS_END = WS_KVP + (size_t)2 * 16 * 16 * KVP_SLOT;

template <int I, int N, class F> DI void sfor(F&& f) { if constexpr (I < N) { f(std::integral_constant<int, I>{}); sfor<I + 1, N>(f); } }
template <int V> using IC = std::integral_constant<int, V>;

typedef __bf16 bf16x2_n __attribute__((ext_vector_type(2)));
DI unsigned pk_bf16(float lo, float hi) { const bf16x2_n v = __builtin_convertvector((f32x2){lo, hi}, bf16x2_n); return __builtin_bit_cast(unsigned, v); }
DI float bf_lo(unsigned u) { return __uint_as_float(u << 16); }
DI float bf_hi(unsigned u) { return __uint_as_float(u & 0xffff0000u); }
DI float xg_sum(float v) { v += __shfl_xor(v, 16); v += __shfl_xor(v, 32); return v; }
DI int lane_id() { return __builtin_amdgcn_mbcnt_hi(~0u, __builtin_amdgcn_mbcnt_lo(~0u, 0u)); }

DI void wait_vm0() { asm volatile("s_waitcnt vmcnt(0)" ::: "memory"); }
template <int N> DI void wait_vm() { asm volatile("s_waitcnt vmcnt(%0)" ::"i"(N) : "memory"); }
template <int N> DI void wait_lgkm() { asm volatile("s_waitcnt lgkmcnt(%0)" ::"i"(N) : "memory"); }
DI void bar() { asm volatile("s_barrier" ::: "memory"); }
template <int OFF> DI void ds_rd(bf16x8& d, unsigned addr) { asm volatile("ds_read_b128 %0, %1 offset:%2" : "=v"(d) : "v"(addr), "i"(OFF)); }
template <int OFF> DI void ld_a(bf16x8& d, const char* sb, unsigned voff) { asm volatile("global_load_dwordx4 %0, %1, %2 offset:%3" : "=a"(d) : "v"(voff), "s"(sb), "i"(OFF)); }
template <int OFF> DI void ld_v(u32x4& d, const char* sb, unsigned voff) { asm volatile("global_load_dwordx4 %0, %1, %2 offset:%3" : "=v"(d) : "v"(voff), "s"(sb), "i"(OFF)); }
DI void ld_v2(u32x2& d, const char* sb, unsigned voff) { asm volatile("global_load_dwordx2 %0, %1, %2" : "=v"(d) : "v"(voff), "s"(sb)); }
DI void ld_f(float& d, const char* sb, unsigned voff) { asm volatile("global_load_dword %0, %1, %2" : "=v"(d) : "v"(voff), "s"(sb)); }
DI void set_m0(unsigned lds) { asm volatile("s_mov_b32 m0, %0\n\ts_nop 0" ::"s"(lds) : "memory"); }
template <int OFF> DI void dma(const char* sb, unsigned voff) { asm volatile("global_load_lds_dwordx4 %0, %1 offset:%2" ::"v"(voff), "s"(sb), "i"(OFF) : "memory"); }
template <int P> DI void dma_piece(const char* sb, unsigned lds, unsigned voff) {
    if constexpr (P % 4 == 0) { set_m0(lds + P * FRAG); }
    dma<(P % 4) * FRAG>(sb + (P / 4) * 4 * FRAG, voff);
}
DI void mma0(f32x4& c, const bf16x8& vb, const bf16x8& aa) { asm volatile("v_mfma_f32_16x16x32_bf16 %0, %1, %2, 0" : "=a"(c) : "v"(vb), "a"(aa)); }
DI void mma(f32x4& c, const bf16x8& vb, const bf16x8& aa) { asm volatile("v_mfma_f32_16x16x32_bf16 %0, %1, %2, %0" : "+a"(c) : "v"(vb), "a"(aa)); }
DI void mma0_t(f32x4& c, const bf16x8& vb, const bf16x8& aa) { asm volatile("v_mfma_f32_16x16x32_bf16 %0, %2, %1, 0" : "=a"(c) : "v"(vb), "a"(aa)); }
DI void mma_t(f32x4& c, const bf16x8& vb, const bf16x8& aa) { asm volatile("v_mfma_f32_16x16x32_bf16 %0, %2, %1, %0" : "+a"(c) : "v"(vb), "a"(aa)); }
DI void mma_fence(f32x4& c) { asm volatile("" : "+a"(c)); }
DI void mma_settle() { asm volatile("s_nop 7\n\ts_nop 7" ::: "memory"); }

DI f32x4 mfma_c(const u32x4& a, const u32x4& b, const f32x4& c) {
    return __builtin_amdgcn_mfma_f32_16x16x32_bf16(__builtin_bit_cast(bf16x8, a), __builtin_bit_cast(bf16x8, b), c, 0, 0, 0);
}
DI float phi(float t) { return (t > 0.f ? t + 1.f : __expf(t)) + 1e-6f; }

__host__ __device__ constexpr int h_tile_base(int f) { return f <= 12 ? 7 * f : 84 + 6 * (f - 12); }

DI void map16x16(int b, int& r, int& c) { const int x = b & 7, q = b >> 3; r = (x >> 2) * 8 + (q & 7); c = (x & 3) * 4 + (q >> 3); }
DI void map32x8(int b, int& r, int& c) { const int x = b & 7, q = b >> 3; r = (x >> 1) * 8 + (q & 7); c = (x & 1) * 4 + (q >> 3); }

constexpr int PREP_UNITS = 320 + 160 + 320 + 432 + 128;
constexpr int PREP_WAVE_LDS = 14336;
struct PrepArgs { const char *x, *wqkv, *wo, *wg, *wu, *wd; char* ws; };

DI bf16x8 tr_frag(unsigned addr, unsigned rs) {
    s16x4 lo, hi;
    asm volatile("ds_read_b64_tr_b16 %0, %2\n\tds_read_b64_tr_b16 %1, %3\n\ts_waitcnt lgkmcnt(0)" : "=&v"(lo), "=&v"(hi) : "v"(addr), "v"(addr + 4 * rs) : "memory");
    return __builtin_shufflevector(lo, hi, 0, 1, 2, 3, 4, 5, 6, 7);
}
DI void prep_store(char* dst, const bf16x8& fr, int lane) { *(GAS bf16x8*)(dst + lane * 16) = fr; }
DI void prep_colsum(float* dst, const bf16x8& fr, int lane) {
    const bf16x8 ones = {0x3f80, 0x3f80, 0x3f80, 0x3f80, 0x3f80, 0x3f80, 0x3f80, 0x3f80};
    const f32x4 z = {0.f, 0.f, 0.f, 0.f};
    const f32x4 d = __builtin_amdgcn_mfma_f32_16x16x32_bf16(fr, ones, z, 0, 0, 0);
    if ((lane & 15) == 0) *(GAS f32x4*)(dst + (lane >> 4) * 4) = d;
}

__global__ __launch_bounds__(256, 2) void prep_kernel(PrepArgs a) {
    extern __shared__ __attribute__((aligned(16))) char shm[];
    const int wid = __builtin_amdgcn_readfirstlane(threadIdx.x >> 6), lane = threadIdx.x & 63;
    int u = blockIdx.x * 4 + wid;
    if (u >= PREP_UNITS) return;
    const int g = lane >> 4, q = (lane & 15) >> 2, p = lane & 3, c16 = lane & 15;
    LAS char* img = (LAS char*)shm + wid * PREP_WAVE_LDS;
    const unsigned imgaddr = (unsigned)(size_t)shm + wid * PREP_WAVE_LDS;
    const bf16x8 zero = {0, 0, 0, 0, 0, 0, 0, 0};
    if (u < 320) {
        const int hp = u / 20, ks = u % 20;
#pragma unroll
        for (int it = 0; it < 8; ++it) {
            int i = it * 64 + lane; if (i >= 480) i = 479;
            const int row = i / 15, cc = i % 15, seg = cc / 5, c5 = cc % 5;
            const char* src = a.wqkv + (size_t)(ks * 32 + row) * 3840 + seg * 1280 + hp * 80 + c5 * 16;
            __builtin_amdgcn_global_load_lds((const GAS unsigned*)src, (LAS unsigned*)(img + it * 1024), 16, 0, 0);
        }
        wait_vm0();
        const unsigned rs = 240, rowaddr = imgaddr + (8 * g + q) * rs;
        char* w1 = a.ws + WS_WQKV + (size_t)hp * 160 * FRAG + (size_t)ks * 5 * FRAG;
        char* w2 = a.ws + WS_WQKV + (size_t)hp * 160 * FRAG + (size_t)(100 + ks * 3) * FRAG;
        float* cs = (float*)(a.ws + WS_CSQ) + (hp * 20 + ks) * 128;
#pragma unroll
        for (int nt = 0; nt < 8; ++nt) {
            int col;
            if (nt == 0) col = 40 + 4 * p; else if (nt == 1) col = 56 + 4 * p;
            else if (nt == 2) col = p < 2 ? 72 + 4 * p : 112 + 4 * (p - 2);
            else if (nt == 3) col = 80 + 4 * p; else if (nt == 4) col = 96 + 4 * p;
            else if (nt == 5) col = 4 * p; else if (nt == 6) col = 20 + 4 * p;
            else col = p == 0 ? 16 : 36;
            bf16x8 fr = tr_frag(rowaddr + col * 2, rs);
            if (nt == 7 && c16 >= 8) fr = zero;
            prep_store(nt < 5 ? w1 + nt * FRAG : w2 + (nt - 5) * FRAG, fr, lane);
            prep_colsum(cs + nt * 16, fr, lane);
        }
        return;
    }
    u -= 320;
    if (u < 160 + 0 || (u >= 480 && u < 912)) {
        const bool down = u >= 480;
        const int v = down ? u - 480 : u, nks = down ? 54 : 20, c8 = v / nks, ks = v % nks;
        const char* w = down ? a.wd : a.wo;
#pragma unroll
        for (int it = 0; it < 5; ++it) {
            const int i = it * 64 + lane, row = i / 10, cc = i % 10;
            int wrow = ks * 32 + row;
            if (down) {
                if (ks < 48) wrow = 16 * (h_tile_base(ks / 3) + 2 * (ks % 3)) + row;
                else { const int f0 = 2 * (ks - 48); wrow = row < 16 ? 16 * (h_tile_base(f0) + 6) + row : 16 * (h_tile_base(f0 + 1) + 6) + row - 16; }
            }
            const char* src = w + (size_t)wrow * 1280 + c8 * 160 + cc * 16;
            __builtin_amdgcn_global_load_lds((const GAS unsigned*)src, (LAS unsigned*)(img + it * 1024), 16, 0, 0);
        }
        wait_vm0();
        const unsigned rs = 160, rowaddr = imgaddr + (8 * g + q) * rs;
        const int half = nks / 2, kh = ks / half, s = ks % half;
        char* dst = a.ws + (down ? WS_WD + (size_t)c8 * 272 * FRAG : WS_WO + (size_t)c8 * 100 * FRAG) + (size_t)(s * 10 + kh * 5) * FRAG;
        const int pa = (c8 & 1) ? 16 : 0, hf = (c8 & 1) ? 0 : 64;
#pragma unroll
        for (int nt = 0; nt < 5; ++nt) {
            const int col = nt < 4 ? pa + 32 * (nt >> 1) + 8 * p + 4 * (nt & 1) : hf + 4 * p;
            prep_store(dst + nt * FRAG, tr_frag(rowaddr + col * 2, rs), lane);
        }
        if (down && ks == 53) { prep_store(dst + 5 * FRAG, zero, lane); prep_store(dst + 6 * FRAG, zero, lane); }
        return;
    }
    if (u < 480) {
        const int v = u - 160, f = v / 20, ks = v % 20, tb = h_tile_base(f), nt7 = f < 12;
#pragma unroll
        for (int it = 0; it < 14; ++it) {
            const int i = it * 64 + lane, row = i / 28, cc = i % 28, up = cc / 14; int c14 = cc % 14;
            if (!nt7 && c14 >= 12) c14 = 0;
            const char* src = (up ? a.wu : a.wg) + (size_t)(ks * 32 + row) * 3456 + tb * 32 + c14 * 16;
            __builtin_amdgcn_global_load_lds((const GAS unsigned*)src, (LAS unsigned*)(img + it * 1024), 16, 0, 0);
        }
        wait_vm0();
        const unsigned rs = 448, rowaddr = imgaddr + (8 * g + q) * rs;
        char* dst = a.ws + WS_WGU + (size_t)f * 280 * FRAG + (size_t)ks * 14 * FRAG;
        float* cs = (float*)(a.ws + WS_CSG) + (f * 20 + ks) * 224;
#pragma unroll
        for (int nt = 0; nt < 14; ++nt) {
            const int j = nt >> 1, up = nt & 1;
            const int col = up * 112 + (j < 6 ? 32 * (j >> 1) + 8 * p + 4 * (j & 1) : 96 + 4 * p);
            bf16x8 fr = tr_frag(rowaddr + col * 2, rs);
            if (j == 6 && !nt7) fr = zero;
            prep_store(dst + nt * FRAG, fr, lane);
            prep_colsum(cs + nt * 16, fr, lane);
        }
        return;
    }
    {
        const int mt = u - 912, r = lane & 15;
        const char* src = a.x + (size_t)(mt * 16 + r) * 1280 + g * 16;
        char* dst = a.ws + WS_X + (size_t)mt * KSD * FRAG + lane * 16;
        u32x4 v[20];
#pragma unroll
        for (int ks = 0; ks < 20; ++ks) v[ks] = *(const GAS u32x4*)(src + ks * 64);
        float s = 0.f, s2 = 0.f;
#pragma unroll
        for (int ks = 0; ks < 20; ++ks) {
            *(GAS u32x4*)(dst + ks * FRAG) = v[ks];
#pragma unroll
            for (int j = 0; j < 4; ++j) { const float lo = bf_lo(v[ks][j]), hi = bf_hi(v[ks][j]); s += lo + hi; s2 += lo * lo + hi * hi; }
        }
        s = xg_sum(s); s2 = xg_sum(s2);
        f32x4 o = {0.f, 0.f, 0.f, 0.f}; if (g == 0) { o[0] = s; o[1] = s2; }
        *(GAS f32x4*)(a.ws + WS_ST + (size_t)(mt * 16 + r) * 64 + g * 16) = o;
    }
}

DI void row_stats(const u32x4& p, float& rstd, float& mrs) {
    float s = __uint_as_float(p[0]) + __uint_as_float(p[2]), s2 = __uint_as_float(p[1]) + __uint_as_float(p[3]);
    s = xg_sum(s); s2 = xg_sum(s2);
    const float mean = s * (1.f / DM), var = fmaxf(s2 * (1.f / DM) - mean * mean, 0.f);
    rstd = __builtin_amdgcn_rsqf(var + 1e-5f);
    mrs = mean * rstd;
}

constexpr int P1_RING = 0, P1_SLOT = 20 * FRAG, P1_SCR = 100 * FRAG, P1_IMG = P1_SCR + 32 * FRAG, P1_TAB = P1_IMG + 6 * FRAG, P1_CS = P1_TAB + 1024, P1_LDS = P1_CS + 512;
struct P1Args { const char* ws_c; char* ws; int xin, layer; };

__global__ __launch_bounds__(256, 1) void p1_kernel(P1Args a) {
    extern __shared__ __attribute__((aligned(16))) char shm[];
    const unsigned lds0 = (unsigned)(size_t)shm;
    LAS char* lds = (LAS char*)shm;
    const int tid = threadIdx.x, wid = __builtin_amdgcn_readfirstlane(tid >> 6), lane = tid & 63, g = lane >> 4, r = lane & 15;
    int rp, hp; map16x16(blockIdx.x, rp, hp);
    const unsigned voff = lane * 16;
    const int mt0 = rp * 8 + wid * 2;
    const char* wsrc = a.ws_c + WS_WQKV + (size_t)hp * 160 * FRAG;
    const char* xsrc = a.ws_c + WS_X + (size_t)a.xin * XT_BYTES + (size_t)mt0 * KSD * FRAG;
    const char* stsrc = a.ws_c + WS_ST + (size_t)a.xin * ST_BYTES + (size_t)mt0 * 16 * 64;

    bf16x8 A[2][20];
    f32x4 acc1[2][5], acc2[2][3];
    u32x4 stp[2]; float csp[20];
    auto dma_chunk_piece = [&](auto C, auto P) {
        constexpr int c = C, p = P;
        constexpr int per = c < 5 ? 5 : 3, off = c < 5 ? c * 20 : 100 + (c - 5) * 12;
        if constexpr (p < per) dma_piece<p>(wsrc + (size_t)(off + wid * per) * FRAG, lds0 + P1_RING + (c % 5) * P1_SLOT + wid * per * FRAG, voff);
    };
    auto dma_chunk = [&](auto C) { sfor<0, 5>([&](auto P) { dma_chunk_piece(C, P); }); };
    auto a_load = [&](auto M, auto KS) { constexpr int m = M, ks = KS; ld_a<(ks % 4) * FRAG>(A[m][ks], xsrc + (size_t)(m * KSD + (ks / 4) * 4) * FRAG, voff); };
    auto a_chunk = [&](auto C) { sfor<0, 4>([&](auto S) { a_load(IC<0>{}, IC<C * 4 + S>{}); a_load(IC<1>{}, IC<C * 4 + S>{}); }); };

    ld_v<0>(stp[0], stsrc, r * 64 + g * 16); ld_v<FRAG>(stp[1], stsrc, r * 64 + g * 16);
    {
        const char* cssrc = a.ws_c + WS_CSQ + (size_t)hp * 20 * 128 * 4;
        sfor<0, 20>([&](auto K) { ld_f(csp[K], cssrc + K * 512, (tid & 127) * 4); });
    }
    dma_chunk(IC<0>{}); a_chunk(IC<0>{});
    dma_chunk(IC<1>{}); a_chunk(IC<1>{});
    dma_chunk(IC<2>{}); a_chunk(IC<2>{});
    for (int i = tid; i < 6 * FRAG / 16; i += 256) *(LAS u32x4*)(lds + P1_IMG + i * 16) = (u32x4){0u, 0u, 0u, 0u};
    wait_vm<39>();
    float rstd[2], mrs[2];
    asm volatile("" : "+v"(stp[0]), "+v"(stp[1]));
    row_stats(stp[0], rstd[0], mrs[0]); row_stats(stp[1], rstd[1], mrs[1]);
    if (g == 0) {
        *(LAS f32x2*)(lds + P1_TAB + (wid * 32 + r) * 8) = (f32x2){rstd[0], mrs[0]};
        *(LAS f32x2*)(lds + P1_TAB + (wid * 32 + 16 + r) * 8) = (f32x2){rstd[1], mrs[1]};
    }
    {
        float s = 0.f;
        sfor<0, 20>([&](auto K) { asm volatile("" : "+v"(csp[K])); s += csp[K]; });
        if (tid < 128) *(LAS float*)(lds + P1_CS + tid * 4) = s;
    }
    wait_lgkm<0>();

    constexpr int L = 4, R = 5;
    bf16x8 B[R];
    const unsigned rd0 = lds0 + P1_RING + voff;
    wait_vm<26>(); bar();
    auto rd1 = [&](auto I) { constexpr int i = I; if constexpr (i < 100) ds_rd<(i % 20) * FRAG>(B[i % R], rd0 + ((i / 20) % 5) * P1_SLOT); };
    auto win1 = [&](auto J, auto W) {
        constexpr int tc = J + 3, wi = W;
        if constexpr (tc <= 7) {
            if constexpr (wi < 5) dma_chunk_piece(IC<tc>{}, IC<wi>{});
            else if constexpr (tc < 5 && wi < 13) a_load(IC<(wi - 5) & 1>{}, IC<tc * 4 + ((wi - 5) >> 1)>{});
        }
    };
    sfor<0, L>([&](auto W) { win1(IC<0>{}, W); });
    sfor<0, L>([&](auto I) { rd1(I); });
    sfor<0, 100>([&](auto I) {
        constexpr int i = I, c = i / 20, ii = i % 20, ks = i / 5, n = i % 5;
        if constexpr (ii == 20 - L && c < 4) { wait_vm<c < 2 ? 26 : (c == 2 ? 16 : 6)>(); bar(); }
        if constexpr (ii >= 20 - L) { if constexpr (c < 4) win1(IC<c + 1>{}, IC<ii - (20 - L)>{}); } else win1(IC<c>{}, IC<ii + L>{});
        rd1(IC<i + L>{});
        if constexpr (i + L < 100) wait_lgkm<L>(); else wait_lgkm<99 - i>();
        if constexpr (ks == 0) { mma0_t(acc1[0][n], B[i % R], A[0][ks]); mma0_t(acc1[1][n], B[i % R], A[1][ks]); }
        else { mma_t(acc1[0][n], B[i % R], A[0][ks]); mma_t(acc1[1][n], B[i % R], A[1][ks]); }
    });
    mma_settle();
    sfor<0, 5>([&](auto N) { mma_fence(acc1[0][N]); mma_fence(acc1[1][N]); });

    {
        u32x4 op[5];
        f32x4 st[2][2];
        sfor<0, 2>([&](auto M) { st[M][0] = *(LAS f32x4*)(lds + P1_TAB + (wid * 32 + M * 16 + 4 * g) * 8); st[M][1] = *(LAS f32x4*)(lds + P1_TAB + (wid * 32 + M * 16 + 4 * g) * 8 + 16); });
        sfor<0, 5>([&](auto N) {
            constexpr int n = N;
            const float cs = *(LAS float*)(lds + P1_CS + (n * 16 + r) * 4);
            float v[2][4];
            sfor<0, 2>([&](auto M) {
                sfor<0, 4>([&](auto J) { constexpr int m = M, j = J;
                    float t = acc1[m][n][j] * st[m][j >> 1][(j & 1) * 2] - st[m][j >> 1][(j & 1) * 2 + 1] * cs;
                    if constexpr (n < 2) t = phi(t); else if constexpr (n == 2) t = r < 8 ? phi(t) : t;
                    v[m][j] = t; });
            });
            op[n] = (u32x4){pk_bf16(v[0][0], v[0][1]), pk_bf16(v[0][2], v[0][3]), pk_bf16(v[1][0], v[1][1]), pk_bf16(v[1][2], v[1][3])};
        });
        u32x4 v2 = op[2];
        if (r < 8) v2 = (u32x4){0x3f803f80u, 0x3f803f80u, 0x3f803f80u, 0x3f803f80u};
        const f32x4 z4 = {0.f, 0.f, 0.f, 0.f};
        f32x4 kv[8];
        kv[0] = mfma_c(op[0], op[3], z4); kv[1] = mfma_c(op[0], op[4], z4); kv[2] = mfma_c(op[0], v2, z4);
        kv[3] = mfma_c(op[1], op[3], z4); kv[4] = mfma_c(op[1], op[4], z4); kv[5] = mfma_c(op[1], v2, z4);
        kv[6] = mfma_c(op[2], op[4], z4); kv[7] = mfma_c(op[2], v2, z4);
        sfor<0, 8>([&](auto T) { *(LAS f32x4*)(lds + P1_SCR + (wid * 8 + T) * FRAG + voff) = kv[T]; });
    }
    asm volatile("s_waitcnt lgkmcnt(0)" ::: "memory"); bar();
    int s_dg = 0, s_e = 0; bool s_z = false;
    if (tid < 200) { const int hd = tid / 100, v = tid % 100; s_dg = hd * 5 + v / 20; s_e = hd * 20 + v % 20; }
    else { s_z = true; s_dg = tid - 200; }
    char* kvp = a.ws + WS_KVP + (size_t)((a.layer * 16 + hp) * 16) * KVP_SLOT;
    if (tid < 210) {
        const int dt = s_dg >> 2, gg = s_dg & 3;
        const int et = s_z ? 2 : (s_e >> 4 == 2 ? 2 : s_e >> 4), cc = s_z ? 0 : (s_e < 32 ? s_e & 15 : s_e - 24);
        const int T = dt == 2 ? 5 + et : dt * 3 + et;
        const unsigned o = P1_SCR + T * FRAG + (gg * 16 + cc) * 16;
        const f32x4 sum = (*(LAS f32x4*)(lds + o) + *(LAS f32x4*)(lds + o + 8 * FRAG)) + (*(LAS f32x4*)(lds + o + 16 * FRAG) + *(LAS f32x4*)(lds + o + 24 * FRAG));
        const __amdgpu_buffer_rsrc_t rs = __builtin_amdgcn_make_buffer_rsrc((void*)kvp, 0, 16 * KVP_SLOT, 0x00020000);
        __builtin_amdgcn_raw_buffer_store_b128(__builtin_bit_cast(u32x4, sum), rs, rp * KVP_SLOT + tid * 16, 0, 16);
    }
    wait_vm0(); bar();
    gu32* cnt = (gu32*)(a.ws + WS_CTL) + CTL_KV + (a.layer * 16 + hp) * 64;
    if (tid == 0) __hip_atomic_fetch_add(cnt, 1u, __ATOMIC_RELAXED, __HIP_MEMORY_SCOPE_AGENT);

    {
        constexpr int R2 = 6, L2 = 4;
        bf16x8 B2[R2];
        auto rd2 = [&](auto I) { constexpr int i = I; if constexpr (i < 60) ds_rd<(i % 12) * FRAG>(B2[i % R2], rd0 + ((5 + i / 12) % 5) * P1_SLOT); };
        sfor<0, 3>([&](auto P) { dma_chunk_piece(IC<8>{}, P); });
        sfor<0, 3>([&](auto P) { dma_chunk_piece(IC<9>{}, P); });
        sfor<0, L2>([&](auto I) { rd2(I); });
        sfor<0, 60>([&](auto I) {
            constexpr int i = I, c = i / 12, ii = i % 12, ks = i / 3, n = i % 3;
            if constexpr (ii == 12 - L2 && c == 2) { wait_vm<3>(); bar(); }
            if constexpr (ii == 12 - L2 && c == 3) { wait_vm<0>(); bar(); }
            rd2(IC<i + L2>{});
            if constexpr (i + L2 < 60) wait_lgkm<L2>(); else wait_lgkm<59 - i>();
            if constexpr (ks == 0) { mma0(acc2[0][n], B2[i % R2], A[0][ks]); mma0(acc2[1][n], B2[i % R2], A[1][ks]); }
            else { mma(acc2[0][n], B2[i % R2], A[0][ks]); mma(acc2[1][n], B2[i % R2], A[1][ks]); }
        });
        mma_settle();
        sfor<0, 3>([&](auto N) { mma_fence(acc2[0][N]); mma_fence(acc2[1][N]); });
    }
    u32x4 qa[2], qb[2];
    sfor<0, 2>([&](auto M) {
        constexpr int m = M;
        unsigned pk[3][2];
        sfor<0, 3>([&](auto N) {
            constexpr int n = N;
            const f32x4 cs = *(LAS f32x4*)(lds + P1_CS + ((5 + n) * 16 + 4 * g) * 4);
            float v[4];
            sfor<0, 4>([&](auto J) { constexpr int j = J; v[j] = phi(acc2[m][n][j] * rstd[m] - mrs[m] * cs[j]); });
            pk[n][0] = pk_bf16(v[0], v[1]); pk[n][1] = pk_bf16(v[2], v[3]);
        });
        qa[m] = (u32x4){pk[0][0], pk[0][1], pk[2][0], pk[2][1]};
        qb[m] = (u32x4){pk[1][0], pk[1][1], pk[2][0], pk[2][1]};
    });

    if (wid == 0) {
        unsigned spins = 0;
        gu32* tmo = (gu32*)(a.ws + WS_CTL) + CTL_TMO;
        while ((unsigned)__builtin_amdgcn_readfirstlane(__hip_atomic_load(cnt, __ATOMIC_RELAXED, __HIP_MEMORY_SCOPE_AGENT)) < 16u) {
            __builtin_amdgcn_s_sleep(1);
            if (++spins > (1u << 20)) { if (lane == 0) __hip_atomic_store(tmo, 1u, __ATOMIC_RELAXED, __HIP_MEMORY_SCOPE_AGENT); break; }
        }
    }
    asm volatile("" ::: "memory"); bar();
    if (tid < 210) {
        const __amdgpu_buffer_rsrc_t rs = __builtin_amdgcn_make_buffer_rsrc((void*)kvp, 0, 16 * KVP_SLOT, 0x00020000);
        u32x4 pv[16];
#pragma unroll
        for (int i = 0; i < 16; ++i) pv[i] = __builtin_amdgcn_raw_buffer_load_b128(rs, i * KVP_SLOT + tid * 16, 0, 16);
        f32x4 s = __builtin_bit_cast(f32x4, pv[0]);
#pragma unroll
        for (int i = 1; i < 16; ++i) s += __builtin_bit_cast(f32x4, pv[i]);
        const u32x2 w = {pk_bf16(s[0], s[1]), pk_bf16(s[2], s[3])};
        const int hd = s_dg >= 5, dl = s_dg - hd * 5;
        const int gg = dl < 4 ? dl : hd, hb = dl < 4 ? 0 : 8;
        if (!s_z) {
            const int e = s_e, T = e >= 32 ? 2 : (e >> 2) & 1, c = e >= 32 ? e - 32 : (e >> 3) * 4 + (e & 3);
            const int img = T == 2 ? 4 : T * 2 + hd;
            *(LAS u32x2*)(lds + P1_IMG + img * FRAG + (gg * 16 + c) * 16 + hb) = w;
        } else {
            *(LAS u32x2*)(lds + P1_IMG + 5 * FRAG + hd * 64 + gg * 16 + hb) = w;
        }
    }
    asm volatile("s_waitcnt lgkmcnt(0)" ::: "memory"); bar();
    {
        u32x4 im[5];
        sfor<0, 5>([&](auto T) { im[T] = *(LAS u32x4*)(lds + P1_IMG + T * FRAG + voff); });
        const u32x4 da = *(LAS u32x4*)(lds + P1_IMG + 5 * FRAG + g * 16), db = *(LAS u32x4*)(lds + P1_IMG + 5 * FRAG + 64 + g * 16);
        const f32x4 z4 = {0.f, 0.f, 0.f, 0.f};
        char* obase = a.ws + WS_O + (size_t)a.layer * XT_BYTES;
        const __amdgpu_buffer_rsrc_t rs = __builtin_amdgcn_make_buffer_rsrc((void*)obase, 0, (int)XT_BYTES, 0x00020000);
        u32x2 t2p[2];
        sfor<0, 2>([&](auto M) {
            constexpr int m = M;
            f32x4 t0 = mfma_c(im[0], qa[m], z4); t0 = mfma_c(im[1], qb[m], t0);
            f32x4 t1 = mfma_c(im[2], qa[m], z4); t1 = mfma_c(im[3], qb[m], t1);
            const f32x4 t2 = mfma_c(im[4], qb[m], z4);
            const f32x4 dA = mfma_c(da, qa[m], z4), dB = mfma_c(db, qb[m], z4);
            const float ia = 1.f / (dA[0] + 1e-6f), ib = 1.f / (dB[0] + 1e-6f);
            const float i0 = g < 3 ? ia : ib, i1 = g < 2 ? ia : ib;
            const u32x4 w = {pk_bf16(t0[0] * i0, t0[1] * i0), pk_bf16(t0[2] * i0, t0[3] * i0), pk_bf16(t1[0] * i1, t1[1] * i1), pk_bf16(t1[2] * i1, t1[3] * i1)};
            const int G = 5 * hp + g;
            __builtin_amdgcn_raw_buffer_store_b128(w, rs, ((mt0 + m) * KSD + (G >> 2)) * FRAG + (G & 3) * 256 + r * 16, 0, 0);
            t2p[m] = (u32x2){pk_bf16(t2[0] * ib, t2[1] * ib), pk_bf16(t2[2] * ib, t2[3] * ib)};
        });
        const auto s0 = __builtin_amdgcn_permlane16_swap(t2p[0][0], t2p[1][0], false, false);
        const auto s1 = __builtin_amdgcn_permlane16_swap(t2p[0][1], t2p[1][1], false, false);
        const u32x4 w = {s0[0], s1[0], s0[1], s1[1]};
        const int G = 5 * hp + 4;
        if (g < 2) __builtin_amdgcn_raw_buffer_store_b128(w, rs, ((mt0 + g) * KSD + (G >> 2)) * FRAG + (G & 3) * 256 + r * 16, 0, 0);
    }
}

struct PRArgs { const char* ws_c; char* ws; char* out; size_t a_off, w_off; int xres, xout; };
constexpr int pr_nops(int ns, int c) { return c >= (ns + 1) / 2 ? 0 : ((2 * c + 1 < ns) ? 9 : 5); }
constexpr int PR_SLOT = 20 * FRAG, PR_XCH = 100 * FRAG, PR_LDS = PR_XCH + 20 * FRAG;

template <int NS, bool FINAL>
__global__ __launch_bounds__(256, 1) void proj_kernel(PRArgs a) {
    extern __shared__ __attribute__((aligned(16))) char shm[];
    const unsigned lds0 = (unsigned)(size_t)shm;
    LAS char* lds = (LAS char*)shm;
    constexpr int KS = 2 * NS, NCH = (NS + 1) / 2, WFR = NS == 10 ? 100 : 272, D = 3;
    const int tid = threadIdx.x, wid = __builtin_amdgcn_readfirstlane(tid >> 6), lane = tid & 63, g = lane >> 4, r = lane & 15;
    const int mh = wid >> 1, kh = wid & 1;
    int rt, c8; map32x8(blockIdx.x, rt, c8);
    const unsigned voff = lane * 16;
    const int mt0 = rt * 4 + mh * 2;
    const char* wsrc = a.ws_c + a.w_off + (size_t)c8 * WFR * FRAG;
    const char* asrc = a.ws_c + a.a_off + (size_t)(mt0 * KS + kh * NS) * FRAG;
    const int mtf = mt0 + kh, odd = c8 & 1;
    const int tA = odd ? (5 * c8 + 1) / 2 : 5 * c8 / 2, tH = odd ? (5 * c8 - 1) / 2 : 5 * c8 / 2 + 2;
    const char* xres = a.ws_c + WS_X + (size_t)a.xres * XT_BYTES + (size_t)mtf * KSD * FRAG;
    const unsigned hoff = tH * FRAG + (odd * 2 + (g >> 1)) * 256 + r * 16 + (g & 1) * 8;
    u32x4 res[2]; u32x2 resh;
    ld_v<0>(res[0], xres + tA * FRAG, voff); ld_v<FRAG>(res[1], xres + tA * FRAG, voff); ld_v2(resh, xres, hoff);

    bf16x8 A[D + 2][2][2];
    f32x4 acc[2][5];
    auto op = [&](auto C, auto K) {
        constexpr int c = C, k = K;
        if constexpr (c < NCH) {
            constexpr int nd = (2 * c + 1 < NS) ? 5 : 3, na = (2 * c + 1 < NS) ? 4 : 2;
            if constexpr (k < nd) dma_piece<k>(wsrc + (size_t)(c * 20 + wid * nd) * FRAG, lds0 + (c % 5) * PR_SLOT + wid * nd * FRAG, voff);
            else if constexpr (k < nd + na) { constexpr int s = (k - nd) >> 1, m = (k - nd) & 1, st = 2 * c + s;
                ld_a<(st % 4) * FRAG>(A[c % 5][s][m], asrc + (size_t)(m * KS + (st / 4) * 4) * FRAG, voff); }
        }
    };
    sfor<0, D>([&](auto C) { sfor<0, 9>([&](auto K) { op(C, K); }); });

    constexpr int L = 4, R = 5, NF = NS * 5;
    bf16x8 B[R];
    const unsigned rd0 = lds0 + voff + kh * 5 * FRAG;
    auto rd = [&](auto I) { constexpr int i = I; if constexpr (i < NF) { constexpr int s = i / 5, c = s / 2; ds_rd<((s & 1) * 10 + i % 5) * FRAG>(B[i % R], rd0 + (c % 5) * PR_SLOT); } };
    wait_vm<pr_nops(NS, 1) + pr_nops(NS, 2)>(); bar();
    sfor<0, L>([&](auto K) { op(IC<D>{}, K); });
    sfor<0, L>([&](auto I) { rd(I); });
    sfor<0, NF>([&](auto I) {
        constexpr int i = I, s = i / 5, n = i % 5, c = s / 2;
        constexpr int clen = (2 * c + 1 < NS) ? 10 : 5, ii = i - c * 10;
        if constexpr (ii == clen - L && c + 1 < NCH) { wait_vm<pr_nops(NS, c + 2) + pr_nops(NS, c + 3)>(); bar(); }
        if constexpr (ii >= clen - L) op(IC<c + 1 + D>{}, IC<ii - (clen - L)>{}); else op(IC<c + D>{}, IC<ii + L>{});
        rd(IC<i + L>{});
        if constexpr (i + L < NF) wait_lgkm<L>(); else wait_lgkm<NF - 1 - i>();
        if constexpr (s == 0) { mma0(acc[0][n], B[i % R], A[c % 5][s & 1][0]); mma0(acc[1][n], B[i % R], A[c % 5][s & 1][1]); }
        else { mma(acc[0][n], B[i % R], A[c % 5][s & 1][0]); mma(acc[1][n], B[i % R], A[c % 5][s & 1][1]); }
    });
    mma_settle();
    sfor<0, 5>([&](auto N) { mma_fence(acc[0][N]); mma_fence(acc[1][N]); });

    f32x4 fin[5];
    sfor<0, 5>([&](auto N) {
        const f32x4 give = kh ? acc[0][N] : acc[1][N];
        *(LAS f32x4*)(lds + PR_XCH + (wid * 5 + N) * FRAG + voff) = give;
    });
    asm volatile("s_waitcnt lgkmcnt(0)" ::: "memory"); bar();
    sfor<0, 5>([&](auto N) {
        const f32x4 keep = kh ? acc[1][N] : acc[0][N];
        fin[N] = keep + *(LAS f32x4*)(lds + PR_XCH + ((wid ^ 1) * 5 + N) * FRAG + voff);
    });
    wait_vm0();
    asm volatile("" : "+v"(res[0]), "+v"(res[1]), "+v"(resh));
    float s1 = 0.f, s2 = 0.f;
    auto fin_pair = [&](const f32x4& lo, const f32x4& hi, const u32x4& x) {
        u32x4 w;
        w[0] = pk_bf16(lo[0] + bf_lo(x[0]), lo[1] + bf_hi(x[0])); w[1] = pk_bf16(lo[2] + bf_lo(x[1]), lo[3] + bf_hi(x[1]));
        w[2] = pk_bf16(hi[0] + bf_lo(x[2]), hi[1] + bf_hi(x[2])); w[3] = pk_bf16(hi[2] + bf_lo(x[3]), hi[3] + bf_hi(x[3]));
        return w;
    };
    const u32x4 wA = fin_pair(fin[0], fin[1], res[0]), wB = fin_pair(fin[2], fin[3], res[1]);
    const u32x2 wH = {pk_bf16(fin[4][0] + bf_lo(resh[0]), fin[4][1] + bf_hi(resh[0])), pk_bf16(fin[4][2] + bf_lo(resh[1]), fin[4][3] + bf_hi(resh[1]))};
    if constexpr (FINAL) {
        char* row = a.out + (size_t)(mtf * 16 + r) * 1280;
        *(GAS u32x4*)(row + tA * 64 + g * 16) = wA;
        *(GAS u32x4*)(row + tA * 64 + 64 + g * 16) = wB;
        *(GAS u32x2*)(row + tH * 64 + odd * 32 + g * 8) = wH;
    } else {
        char* xo = a.ws + WS_X + (size_t)a.xout * XT_BYTES + (size_t)mtf * KSD * FRAG;
        *(GAS u32x4*)(xo + tA * FRAG + voff) = wA;
        *(GAS u32x4*)(xo + (tA + 1) * FRAG + voff) = wB;
        *(GAS u32x2*)(xo + hoff) = wH;
        auto accum = [&](unsigned u) { const float lo = bf_lo(u), hi = bf_hi(u); s1 += lo + hi; s2 += lo * lo + hi * hi; };
        sfor<0, 4>([&](auto J) { constexpr int j = J; accum(wA[j]); accum(wB[j]); });
        accum(wH[0]); accum(wH[1]);
        s1 = xg_sum(s1); s2 = xg_sum(s2);
        if (g == 0) *(GAS f32x2*)(a.ws + WS_ST + (size_t)a.xout * ST_BYTES + (size_t)(mtf * 16 + r) * 64 + c8 * 8) = (f32x2){s1, s2};
    }
}

struct P3Args { const char* ws_c; char* ws; int xin, layer; };
constexpr int P3_SLOT = 28 * FRAG, P3_CS = 112 * FRAG, P3_LDS = P3_CS + 1024;

__global__ __launch_bounds__(256, 1) void p3_kernel(P3Args a) {
    extern __shared__ __attribute__((aligned(16))) char shm[];
    const unsigned lds0 = (unsigned)(size_t)shm;
    LAS char* lds = (LAS char*)shm;
    const int tid = threadIdx.x, wid = __builtin_amdgcn_readfirstlane(tid >> 6), lane = tid & 63, g = lane >> 4, r = lane & 15;
    int rp, f; map16x16(blockIdx.x, rp, f);
    const unsigned voff = lane * 16;
    const int mt0 = rp * 8 + wid * 2;
    const char* wsrc = a.ws_c + WS_WGU + (size_t)f * 280 * FRAG;
    const char* xsrc = a.ws_c + WS_X + (size_t)a.xin * XT_BYTES + (size_t)mt0 * KSD * FRAG;
    const char* stsrc = a.ws_c + WS_ST + (size_t)a.xin * ST_BYTES + (size_t)mt0 * 16 * 64;

    bf16x8 A[4][2][2];
    f32x4 acc[2][14];
    u32x4 stp[2]; float csp[20];
    auto op = [&](auto C, auto K) {
        constexpr int c = C, k = K;
        if constexpr (c < 10) {
            if constexpr (k < 7) dma_piece<k>(wsrc + (size_t)(c * 28 + wid * 7) * FRAG, lds0 + (c % 4) * P3_SLOT + wid * 7 * FRAG, voff);
            else if constexpr (k < 11) { constexpr int s = (k - 7) >> 1, m = (k - 7) & 1, st = 2 * c + s;
                ld_a<(st % 4) * FRAG>(A[c % 4][s][m], xsrc + (size_t)(m * KSD + (st / 4) * 4) * FRAG, voff); }
        }
    };
    ld_v<0>(stp[0], stsrc, r * 64 + g * 16); ld_v<FRAG>(stp[1], stsrc, r * 64 + g * 16);
    {
        const char* cssrc = a.ws_c + WS_CSG + (size_t)f * 20 * 224 * 4;
        const unsigned co = (tid < 224 ? tid : 0) * 4;
        sfor<0, 20>([&](auto K) { ld_f(csp[K], cssrc + K * 896, co); });
    }
    sfor<0, 2>([&](auto C) { sfor<0, 11>([&](auto K) { op(C, K); }); });
    wait_vm<22>();
    float rstd[2], mrs[2];
    asm volatile("" : "+v"(stp[0]), "+v"(stp[1]));
    row_stats(stp[0], rstd[0], mrs[0]); row_stats(stp[1], rstd[1], mrs[1]);
    {
        float s = 0.f;
        sfor<0, 20>([&](auto K) { asm volatile("" : "+v"(csp[K])); s += csp[K]; });
        if (tid < 224) *(LAS float*)(lds + P3_CS + tid * 4) = s;
    }
    wait_lgkm<0>();

    constexpr int L = 6, R = 7, NF = 280;
    bf16x8 B[R];
    const unsigned rd0 = lds0 + voff;
    auto rd = [&](auto I) { constexpr int i = I; if constexpr (i < NF) ds_rd<(i % 28) * FRAG>(B[i % R], rd0 + ((i / 28) % 4) * P3_SLOT); };
    wait_vm<11>(); bar();
    sfor<0, 11>([&](auto K) { op(IC<2>{}, K); });
    sfor<0, L>([&](auto I) { rd(I); });
    sfor<0, NF>([&](auto I) {
        constexpr int i = I, c = i / 28, ii = i % 28, s = ii / 14, n = ii % 14;
        if constexpr (ii == 28 - L && c < 9) { if constexpr (c + 2 < 10) wait_vm<11>(); else wait_vm<0>(); bar(); }
        constexpr int wj = ii >= 28 - L ? c + 1 : c, wi = ii >= 28 - L ? ii - (28 - L) : ii + L;
        if constexpr (wj >= 1 && wj + 2 < 10 && wi % 2 == 0 && wi / 2 < 11) op(IC<wj + 2>{}, IC<wi / 2>{});
        rd(IC<i + L>{});
        if constexpr (i + L < NF) wait_lgkm<L>(); else wait_lgkm<NF - 1 - i>();
        if constexpr (c == 0 && s == 0) { mma0(acc[0][n], B[i % R], A[c % 4][s][0]); mma0(acc[1][n], B[i % R], A[c % 4][s][1]); }
        else { mma(acc[0][n], B[i % R], A[c % 4][s][0]); mma(acc[1][n], B[i % R], A[c % 4][s][1]); }
    });
    mma_settle();
    sfor<0, 14>([&](auto N) { mma_fence(acc[0][N]); mma_fence(acc[1][N]); });

    char* hb = a.ws + WS_H + (size_t)a.layer * HT_BYTES;
    u32x2 pk[2][7];
    sfor<0, 7>([&](auto J) {
        constexpr int j = J;
        const f32x4 cg = *(LAS f32x4*)(lds + P3_CS + ((2 * j) * 16 + 4 * g) * 4), cu = *(LAS f32x4*)(lds + P3_CS + ((2 * j + 1) * 16 + 4 * g) * 4);
        sfor<0, 2>([&](auto M) {
            constexpr int m = M;
            float h[4];
            sfor<0, 4>([&](auto E) { constexpr int e = E; h[e] = (acc[m][2 * j][e] * rstd[m] - mrs[m] * cg[e]) * (acc[m][2 * j + 1][e] * rstd[m] - mrs[m] * cu[e]); });
            pk[m][j] = (u32x2){pk_bf16(h[0], h[1]), pk_bf16(h[2], h[3])};
        });
        if constexpr (j < 6 && (j & 1)) {
            sfor<0, 2>([&](auto M) {
                const u32x4 w = {pk[M][j - 1][0], pk[M][j - 1][1], pk[M][j][0], pk[M][j][1]};
                *(GAS u32x4*)(hb + (size_t)((mt0 + M) * KSF + 3 * f + (j >> 1)) * FRAG + voff) = w;
            });
        }
    });
    if (f < 12) {
        const auto s0 = __builtin_amdgcn_permlane16_swap(pk[0][6][0], pk[1][6][0], false, false);
        const auto s1 = __builtin_amdgcn_permlane16_swap(pk[0][6][1], pk[1][6][1], false, false);
        const u32x4 w = {s0[0], s1[0], s0[1], s1[1]};
        *(GAS u32x4*)(hb + (size_t)((mt0 + (g & 1)) * KSF + 48 + (f >> 1)) * FRAG + (2 * (f & 1) + (g >> 1)) * 256 + r * 16) = w;
    }
}

extern "C" void kernel_launch(void* const* d_in, const int* in_sizes, int n_in, void* d_out, int out_size, void* d_ws, size_t ws_size, hipStream_t stream) {
    char* ws = (char*)d_ws;
    (void)hipFuncSetAttribute((const void*)prep_kernel, hipFuncAttributeMaxDynamicSharedMemorySize, 4 * PREP_WAVE_LDS);
    (void)hipFuncSetAttribute((const void*)p1_kernel, hipFuncAttributeMaxDynamicSharedMemorySize, P1_LDS);
    (void)hipFuncSetAttribute((const void*)proj_kernel<10, false>, hipFuncAttributeMaxDynamicSharedMemorySize, PR_LDS);
    (void)hipFuncSetAttribute((const void*)proj_kernel<27, false>, hipFuncAttributeMaxDynamicSharedMemorySize, PR_LDS);
    (void)hipFuncSetAttribute((const void*)proj_kernel<27, true>, hipFuncAttributeMaxDynamicSharedMemorySize, PR_LDS);
    (void)hipFuncSetAttribute((const void*)p3_kernel, hipFuncAttributeMaxDynamicSharedMemorySize, P3_LDS);
    (void)hipMemsetAsync(ws + WS_CTL, 0, CTL_BYTES, stream);
    PrepArgs pa{(const char*)d_in[0], (const char*)d_in[1], (const char*)d_in[2], (const char*)d_in[4], (const char*)d_in[5], (const char*)d_in[6], ws};
    hipLaunchKernelGGL(prep_kernel, dim3(PREP_UNITS / 4), dim3(256), 4 * PREP_WAVE_LDS, stream, pa);
    for (int l = 0; l < 2; ++l) {
        P1Args a1{ws, ws, 2 * l, l};
        hipLaunchKernelGGL(p1_kernel, dim3(256), dim3(256), P1_LDS, stream, a1);
        PRArgs a2{ws, ws, (char*)d_out, WS_O + (size_t)l * XT_BYTES, WS_WO, 2 * l, 2 * l + 1};
        hipLaunchKernelGGL((proj_kernel<10, false>), dim3(256), dim3(256), PR_LDS, stream, a2);
        P3Args a3{ws, ws, 2 * l + 1, l};
        hipLaunchKernelGGL(p3_kernel, dim3(256), dim3(256), P3_LDS, stream, a3);
        PRArgs a4{ws, ws, (char*)d_out, WS_H + (size_t)l * HT_BYTES, WS_WD, 2 * l + 1, l == 0 ? 2 : 3};
        if (l == 0) hipLaunchKernelGGL((proj_kernel<27, false>), dim3(256), dim3(256), PR_LDS, stream, a4);
        else hipLaunchKernelGGL((proj_kernel<27, true>), dim3(256), dim3(256), PR_LDS, stream, a4);
    }
}
```

```cpp
#include <hip/hip_runtime.h>
#include <hip/hip_bf16.h>
#include <type_traits>

#define LAS __attribute__((address_space(3)))
#define GAS __attribute__((address_space(1)))
#define DI __device__ __forceinline__
typedef short bf16x8 __attribute__((ext_vector_type(8)));
typedef short s16x4 __attribute__((ext_vector_type(4)));
typedef float f32x4 __attribute__((ext_vector_type(4)));
typedef float f32x2 __attribute__((ext_vector_type(2)));
typedef unsigned u32x4 __attribute__((ext_vector_type(4)));
typedef unsigned u32x2 __attribute__((ext_vector_type(2)));
typedef __bf16 bf16x2_n __attribute__((ext_vector_type(2)));
typedef GAS unsigned gu32;

constexpr int SEQ = 2048, DM = 640;
constexpr int KSD = 20, KSF = 54, NMT = 128;
constexpr int FRAG = 1024;

constexpr size_t WS_CTL = 0;
constexpr int CTL_LINES = 256;
constexpr int C_TMO = 0, C_KV = 1, C_O = 33, C_X1 = 65, C_HA = 97, C_HB = 129, C_X2 = 161;
constexpr size_t CTL_BYTES = (size_t)CTL_LINES * 256;
constexpr size_t WS_WQKV = CTL_BYTES;
constexpr size_t WS_WO = WS_WQKV + (size_t)16 * 160 * FRAG;
constexpr size_t WS_WGU = WS_WO + (size_t)8 * 100 * FRAG;
constexpr size_t WS_WD = WS_WGU + (size_t)16 * 280 * FRAG;
constexpr size_t WS_CSQ = WS_WD + (size_t)8 * 280 * FRAG;
constexpr size_t WS_CSG = WS_CSQ + (size_t)16 * 128 * 20 * 4;
constexpr size_t XT_BYTES = (size_t)NMT * KSD * FRAG;
constexpr size_t WS_X = WS_CSG + (size_t)16 * 224 * 20 * 4;
constexpr size_t ST_BYTES = (size_t)SEQ * 64;
constexpr size_t WS_ST = WS_X + 4 * XT_BYTES;
constexpr size_t WS_O = WS_ST + 4 * ST_BYTES;
constexpr size_t HT_BYTES = (size_t)NMT * KSF * FRAG;
constexpr size_t WS_H = WS_O + 2 * XT_BYTES;
constexpr size_t KVP_SLOT = 3584;
constexpr size_t WS_KVP = WS_H + 2 * HT_BYTES;
constexpr size_t WS_DUMMY = WS_KVP + (size_t)2 * 16 * 16 * KVP_SLOT;
constexpr size_t WS_END = WS_DUMMY + (size_t)1024 * FRAG;

template <int I, int N, class F> DI void sfor(F&& f) { if constexpr (I < N) { f(std::integral_constant<int, I>{}); sfor<I + 1, N>(f); } }
template <int V> using IC = std::integral_constant<int, V>;

DI unsigned pk_bf16(float lo, float hi) { const bf16x2_n v = __builtin_convertvector((f32x2){lo, hi}, bf16x2_n); return __builtin_bit_cast(unsigned, v); }
DI float bf_lo(unsigned u) { return __uint_as_float(u << 16); }
DI float bf_hi(unsigned u) { return __uint_as_float(u & 0xffff0000u); }
DI float xg_sum(float v) {
    const unsigned u = __float_as_uint(v);
    const auto a = __builtin_amdgcn_permlane16_swap(u, u, false, false);
    const float s = __uint_as_float(a[0]) + __uint_as_float(a[1]);
    const unsigned w = __float_as_uint(s);
    const auto b = __builtin_amdgcn_permlane32_swap(w, w, false, false);
    return __uint_as_float(b[0]) + __uint_as_float(b[1]);
}

template <int N> DI void wait_vm() { static_assert(N >= 0 && N < 64, "vmcnt range"); asm volatile("s_waitcnt vmcnt(%0)" ::"i"(N) : "memory"); }
template <int N> DI void wait_lgkm() { static_assert(N >= 0 && N < 16, "lgkmcnt range"); asm volatile("s_waitcnt lgkmcnt(%0)" ::"i"(N) : "memory"); }
DI void bar() { asm volatile("s_barrier" ::: "memory"); }
template <int OFF> DI void ds_rd(bf16x8& d, unsigned addr) { asm volatile("ds_read_b128 %0, %1 offset:%2" : "=v"(d) : "v"(addr), "i"(OFF) : "memory"); }
template <int T> DI void ds_rd_at(bf16x8& d, unsigned lo, unsigned hi) { ds_rd<T % 65536>(d, T < 65536 ? lo : hi); }
template <int OFF> DI void ld_a(bf16x8& d, const char* sb, unsigned voff) { asm volatile("global_load_dwordx4 %0, %1, %2 offset:%3 sc1" : "=a"(d) : "v"(voff), "s"(sb), "i"(OFF) : "memory"); }
template <int OFF> DI void ld_v(u32x4& d, const char* sb, unsigned voff) { asm volatile("global_load_dwordx4 %0, %1, %2 offset:%3 sc1" : "=v"(d) : "v"(voff), "s"(sb), "i"(OFF) : "memory"); }
DI void ld_v2(u32x2& d, const char* sb, unsigned voff) { asm volatile("global_load_dwordx2 %0, %1, %2 sc1" : "=v"(d) : "v"(voff), "s"(sb) : "memory"); }
DI void ld_v1(unsigned& d, const char* sb, unsigned voff) { asm volatile("global_load_dword %0, %1, %2 sc1" : "=v"(d) : "v"(voff), "s"(sb) : "memory"); }
DI void st_v(const char* sb, unsigned voff, const u32x4& v) { asm volatile("global_store_dwordx4 %0, %1, %2 sc1\n\ts_nop 1" ::"v"(voff), "v"(v), "s"(sb) : "memory"); }
DI void st_v2(const char* sb, unsigned voff, const u32x2& v) { asm volatile("global_store_dwordx2 %0, %1, %2 sc1\n\ts_nop 1" ::"v"(voff), "v"(v), "s"(sb) : "memory"); }
DI void set_m0(unsigned lds) { asm volatile("s_mov_b32 m0, %0\n\ts_nop 0" ::"s"(lds) : "memory"); }
template <int OFF> DI void dma(const char* sb, unsigned voff) { asm volatile("global_load_lds_dwordx4 %0, %1 offset:%2" ::"v"(voff), "s"(sb), "i"(OFF) : "memory"); }
template <int P> DI void dma_piece(const char* sb, unsigned lds, unsigned voff) {
    if constexpr (P % 4 == 0) { set_m0(lds + P * FRAG); }
    dma<(P % 4) * FRAG>(sb + (P / 4) * 4 * FRAG, voff);
}
template <bool FIRST> DI void mma(f32x4& c, const bf16x8& w, const bf16x8& x) {
    if constexpr (FIRST) asm volatile("v_mfma_f32_16x16x32_bf16 %0, %1, %2, 0" : "=v"(c) : "v"(w), "a"(x));
    else asm volatile("v_mfma_f32_16x16x32_bf16 %0, %1, %2, %0" : "+v"(c) : "v"(w), "a"(x));
}
template <bool FIRST> DI void mma_t(f32x4& c, const bf16x8& w, const bf16x8& x) {
    if constexpr (FIRST) asm volatile("v_mfma_f32_16x16x32_bf16 %0, %2, %1, 0" : "=v"(c) : "v"(w), "a"(x));
    else asm volatile("v_mfma_f32_16x16x32_bf16 %0, %2, %1, %0" : "+v"(c) : "v"(w), "a"(x));
}
DI void mma_settle() { asm volatile("s_nop 7\n\ts_nop 7" ::: "memory"); }
DI void mma_fence(f32x4& c) { asm volatile("" : "+v"(c)); }
template <class T> DI void pins(T& x) { asm volatile("" : "+s"(x)); }
template <class T> DI void pin(T& x) { asm volatile("" : "+v"(x)); }

DI f32x4 mfma_c(const u32x4& a, const u32x4& b, const f32x4& c) {
    return __builtin_amdgcn_mfma_f32_16x16x32_bf16(__builtin_bit_cast(bf16x8, a), __builtin_bit_cast(bf16x8, b), c, 0, 0, 0);
}
DI float phi(float t) { return (t > 0.f ? t + 1.f : __expf(t)) + 1e-6f; }

__host__ __device__ constexpr int h_tile_base(int f) { return f <= 12 ? 7 * f : 84 + 6 * (f - 12); }
__host__ __device__ constexpr int h_pos_a(int f, int p) { return ((2 * f + p) / 16) * 27 + (2 * f + p) % 16; }
__host__ __device__ constexpr int h_pos_b(int ib) { return (ib / 11) * 27 + 16 + ib % 11; }

#define STAMP(i)

DI void map16x16(int b, int& r, int& c) { const int x = b & 7, q = b >> 3; r = (x >> 2) * 8 + (q & 7); c = (x & 3) * 4 + (q >> 3); }
DI void map32x8(int b, int& r, int& c) { const int x = b & 7, q = b >> 3; r = (x >> 1) * 8 + (q & 7); c = (x & 1) * 4 + (q >> 3); }

constexpr int PREP_UNITS = 320 + 160 + 320 + 432 + 128;
constexpr int PREP_WAVE_LDS = 14336;
struct PrepArgs { const char *x, *wqkv, *wo, *wg, *wu, *wd; char* ws; };

template <int N> DI void tr_frags(bf16x8 (&fr)[N], const unsigned (&addr)[N], unsigned rs) {
    s16x4 lo[N], hi[N];
#pragma unroll
    for (int i = 0; i < N; ++i) asm volatile("ds_read_b64_tr_b16 %0, %2\n\tds_read_b64_tr_b16 %1, %3" : "=&v"(lo[i]), "=&v"(hi[i]) : "v"(addr[i]), "v"(addr[i] + 4 * rs) : "memory");
    asm volatile("s_waitcnt lgkmcnt(0)" ::: "memory");
#pragma unroll
    for (int i = 0; i < N; ++i) { asm volatile("" : "+v"(lo[i]), "+v"(hi[i])); fr[i] = __builtin_shufflevector(lo[i], hi[i], 0, 1, 2, 3, 4, 5, 6, 7); }
}
DI void prep_store(char* dst, const bf16x8& fr, int lane) { *(GAS bf16x8*)(dst + lane * 16) = fr; }
DI void prep_colsum(float* dst, const bf16x8& fr, int lane) {
    const bf16x8 ones = {0x3f80, 0x3f80, 0x3f80, 0x3f80, 0x3f80, 0x3f80, 0x3f80, 0x3f80};
    const f32x4 z = {0.f, 0.f, 0.f, 0.f};
    const f32x4 d = __builtin_amdgcn_mfma_f32_16x16x32_bf16(fr, ones, z, 0, 0, 0);
    const int j = lane & 15;
    if (j < 4) *(GAS float*)(dst + ((lane >> 4) * 4 + j) * 20) = j == 0 ? d[0] : j == 1 ? d[1] : j == 2 ? d[2] : d[3];
}

__global__ __launch_bounds__(256, 2) void prep_kernel(PrepArgs a) {
    extern __shared__ __attribute__((aligned(16))) char shm[];
    const int wid = __builtin_amdgcn_readfirstlane(threadIdx.x >> 6), lane = threadIdx.x & 63;
    if (blockIdx.x == 0) for (int i = threadIdx.x; i < CTL_LINES; i += 256) *(GAS unsigned*)(a.ws + WS_CTL + i * 256) = 0u;
    int u = blockIdx.x * 4 + wid;
    if (u >= PREP_UNITS) return;
    const int g = lane >> 4, q = (lane & 15) >> 2, p = lane & 3, c16 = lane & 15;
    LAS char* img = (LAS char*)shm + wid * PREP_WAVE_LDS;
    const unsigned imgaddr = (unsigned)(size_t)shm + wid * PREP_WAVE_LDS;
    const bf16x8 zero = {0, 0, 0, 0, 0, 0, 0, 0};
    if (u < 128) {
        const int mt = u, r = lane & 15;
        const char* src = a.x + (size_t)(mt * 16 + r) * 1280 + g * 16;
        char* dst = a.ws + WS_X + (size_t)mt * KSD * FRAG + lane * 16;
        u32x4 v[20];
#pragma unroll
        for (int ks = 0; ks < 20; ++ks) v[ks] = *(const GAS u32x4*)(src + ks * 64);
        float s = 0.f, s2 = 0.f;
#pragma unroll
        for (int ks = 0; ks < 20; ++ks) {
            *(GAS u32x4*)(dst + ks * FRAG) = v[ks];
#pragma unroll
            for (int j = 0; j < 4; ++j) { const float lo = bf_lo(v[ks][j]), hi = bf_hi(v[ks][j]); s += lo + hi; s2 += lo * lo + hi * hi; }
        }
        s = xg_sum(s); s2 = xg_sum(s2);
        f32x4 o = {0.f, 0.f, 0.f, 0.f}; if (g == 0) { o[0] = s; o[1] = s2; }
        *(GAS f32x4*)(a.ws + WS_ST + (size_t)(mt * 16 + r) * 64 + g * 16) = o;
        return;
    }
    u -= 128;
    if (u < 320) {
        const int hp = u / 20, ks = u % 20;
#pragma unroll
        for (int it = 0; it < 8; ++it) {
            int i = it * 64 + lane; if (i >= 480) i = 479;
            const int row = i / 15, cc = i % 15, seg = cc / 5, c5 = cc % 5;
            const char* src = a.wqkv + (size_t)(ks * 32 + row) * 3840 + seg * 1280 + hp * 80 + c5 * 16;
            __builtin_amdgcn_global_load_lds((const GAS unsigned*)src, (LAS unsigned*)(img + it * 1024), 16, 0, 0);
        }
        const unsigned rs = 240, rowaddr = imgaddr + (8 * g + q) * rs;
        char* w1 = a.ws + WS_WQKV + (size_t)hp * 160 * FRAG + (size_t)ks * 5 * FRAG;
        char* w2 = a.ws + WS_WQKV + (size_t)hp * 160 * FRAG + (size_t)(100 + ks * 3) * FRAG;
        float* cs = (float*)(a.ws + WS_CSQ) + hp * 128 * 20 + ks;
        const unsigned ad[8] = {rowaddr + 2 * (40 + 4 * p), rowaddr + 2 * (56 + 4 * p), rowaddr + 2 * (p < 2 ? 72 + 4 * p : 112 + 4 * (p - 2)), rowaddr + 2 * (80 + 4 * p),
                                rowaddr + 2 * (96 + 4 * p), rowaddr + 2 * (4 * p), rowaddr + 2 * (20 + 4 * p), rowaddr + 2 * (p == 0 ? 16 : 36)};
        bf16x8 fr[8];
        wait_vm<0>();
        tr_frags<8>(fr, ad, rs);
        if (c16 >= 8) fr[7] = zero;
#pragma unroll
        for (int nt = 0; nt < 8; ++nt) { prep_store(nt < 5 ? w1 + nt * FRAG : w2 + (nt - 5) * FRAG, fr[nt], lane); prep_colsum(cs + nt * 16 * 20, fr[nt], lane); }
        return;
    }
    u -= 320;
    if (u < 320) {
        const int f = u / 20, ks = u % 20, tb = h_tile_base(f), nt7 = f < 12;
#pragma unroll
        for (int it = 0; it < 14; ++it) {
            const int i = it * 64 + lane, row = i / 28, cc = i % 28, up = cc / 14; int c14 = cc % 14;
            if (!nt7 && c14 >= 12) c14 = 0;
            const char* src = (up ? a.wu : a.wg) + (size_t)(ks * 32 + row) * 3456 + tb * 32 + c14 * 16;
            __builtin_amdgcn_global_load_lds((const GAS unsigned*)src, (LAS unsigned*)(img + it * 1024), 16, 0, 0);
        }
        const unsigned rs = 448, rowaddr = imgaddr + (8 * g + q) * rs;
        char* da = a.ws + WS_WGU + (size_t)f * 280 * FRAG + (size_t)ks * 8 * FRAG;
        char* db = a.ws + WS_WGU + (size_t)f * 280 * FRAG + (size_t)(160 + ks * 6) * FRAG;
        float* cs = (float*)(a.ws + WS_CSG) + f * 224 * 20 + ks;
        unsigned ad[14];
#pragma unroll
        for (int nt = 0; nt < 14; ++nt) { const int j = nt >> 1, up = nt & 1; ad[nt] = rowaddr + 2 * (up * 112 + (j < 6 ? 32 * (j >> 1) + 8 * p + 4 * (j & 1) : 96 + 4 * p)); }
        bf16x8 fr[14];
        wait_vm<0>();
        tr_frags<14>(fr, ad, rs);
        if (!nt7) { fr[12] = zero; fr[13] = zero; }
#pragma unroll
        for (int nt = 0; nt < 14; ++nt) { prep_store(nt < 8 ? da + nt * FRAG : db + (nt - 8) * FRAG, fr[nt], lane); prep_colsum(cs + nt * 16 * 20, fr[nt], lane); }
        return;
    }
    u -= 320;
    {
        const bool down = u >= 160;
        const int v = down ? u - 160 : u, nks = down ? 54 : 20, c8 = v / nks, ks = v % nks;
        const char* w = down ? a.wd : a.wo;
        int row_lo = ks * 32, row_hi = ks * 32 + 16;
        if (down) {
            const int kh = ks / 27, s = ks % 27;
            if (s < 16) { const int ia = kh * 16 + s; row_lo = 16 * (h_tile_base(ia >> 1) + 2 * (ia & 1)); row_hi = row_lo + 16; }
            else { const int ib = kh * 11 + s - 16;
                if (ib < 16) { row_lo = 16 * (h_tile_base(ib) + 4); row_hi = row_lo + 16; }
                else { const int f0 = 2 * (ib - 16); row_lo = 16 * (h_tile_base(f0) + 6); row_hi = 16 * (h_tile_base(f0 + 1) + 6); } }
        }
#pragma unroll
        for (int it = 0; it < 5; ++it) {
            const int i = it * 64 + lane, row = i / 10, cc = i % 10;
            const int wrow = row < 16 ? row_lo + row : row_hi + row - 16;
            const char* src = w + (size_t)wrow * 1280 + c8 * 160 + cc * 16;
            __builtin_amdgcn_global_load_lds((const GAS unsigned*)src, (LAS unsigned*)(img + it * 1024), 16, 0, 0);
        }
        const unsigned rs = 160, rowaddr = imgaddr + (8 * g + q) * rs;
        const int half = nks / 2, kh = ks / half, s = ks % half;
        char* dst = a.ws + (down ? WS_WD + (size_t)c8 * 280 * FRAG : WS_WO + (size_t)c8 * 100 * FRAG) + (size_t)(s * 10 + kh * 5) * FRAG;
        const int pa = (c8 & 1) ? 16 : 0, hf = (c8 & 1) ? 0 : 64;
        unsigned ad[5];
#pragma unroll
        for (int nt = 0; nt < 5; ++nt) ad[nt] = rowaddr + 2 * (nt < 4 ? pa + 32 * (nt >> 1) + 8 * p + 4 * (nt & 1) : hf + 4 * p);
        bf16x8 fr[5];
        wait_vm<0>();
        tr_frags<5>(fr, ad, rs);
#pragma unroll
        for (int nt = 0; nt < 5; ++nt) prep_store(dst + nt * FRAG, fr[nt], lane);
    }
}

enum { K_A = 0, K_W = 1, K_X = 2 };
struct Op { int pos, kind, a, b; };
template <int N> struct Sched {
    Op op[N] = {}; int n = 0;
    constexpr void add(int pos, int kind, int a, int b) { op[n] = Op{pos, kind, a, b}; ++n; }
    constexpr void sort() { for (int i = 1; i < n; ++i) { const Op t = op[i]; int j = i - 1; while (j >= 0 && op[j].pos > t.pos) { op[j + 1] = op[j]; --j; } op[j + 1] = t; } }
    constexpr int before(int pos) const { int c = 0; for (int i = 0; i < n; ++i) c += op[i].pos < pos; return c; }
    constexpr int find(int kind, int a, int b) const { for (int i = 0; i < n; ++i) if (op[i].kind == kind && op[i].a == a && op[i].b == b) return i; return -1; }
    constexpr int wait(int pos, int kind, int a, int b) const { const int i = find(kind, a, b); if (i < 0) return 63; const int w = before(pos) - i - 1; return w < 0 ? -1 : (w > 63 ? 63 : w); }
};
constexpr int cmin(int a, int b) { return a < b ? a : b; }

constexpr int NSLOT = 6, SLOT = 20 * FRAG;
constexpr int V_P1 = 0, V_P2 = 8, V_P3 = 14, V_P4 = 28, V_END = 42;
constexpr int slot_of(int v) { return (v % NSLOT) * SLOT; }

enum { X_STAT = 0, X_KVST = 1, X_HST = 2, X_FLAG = 3 };

constexpr Sched<96> make_p1() {
    Sched<96> s;
    for (int k = 0; k < 8; ++k) { s.add(-1, K_A, k, 0); s.add(-1, K_A, k, 1); }
    s.add(-1, K_X, X_STAT, 0); s.add(-1, K_X, X_STAT, 1);
    for (int k = 8; k < 20; ++k) { s.add(5 * (k - 8), K_A, k, 0); s.add(5 * (k - 8) + 2, K_A, k, 1); }
    for (int c = 0; c < 3; ++c) for (int p = 0; p < 5; ++p) s.add(61 + (c * 5 + p) * 5 / 2, K_W, 6 + c, p);
    s.add(99, K_X, X_KVST, 0);
    for (int c = 0; c < 3; ++c) for (int p = 0; p < 5; ++p) s.add(100 + c * 20 + (c == 0 ? 0 : -4) + p * 3, K_W, 9 + c, p);
    s.sort(); return s;
}
constexpr Sched<128> make_p2() {
    Sched<128> s;
    for (int k = 0; k < 6; ++k) { s.add(-1, K_A, k, 0); s.add(-1, K_A, k, 1); }
    for (int k = 6; k < 10; ++k) { s.add(5 * (k - 6), K_A, k, 0); s.add(5 * (k - 6) + 2, K_A, k, 1); }
    for (int c = 2; c < 5; ++c) for (int p = 0; p < 5; ++p) s.add(10 * c - 4 + p * 2, K_W, V_P2 + c + 4, p);
    s.sort(); return s;
}
constexpr Sched<128> make_p3() {
    Sched<128> s;
    for (int k = 0; k < 6; ++k) { s.add(-1, K_A, k, 0); s.add(-1, K_A, k, 1); }
    s.add(-1, K_X, X_STAT, 0); s.add(-1, K_X, X_STAT, 1);
    for (int k = 6; k < 20; ++k) { s.add(8 * (k - 6) + 1, K_A, k, 0); s.add(8 * (k - 6) + 5, K_A, k, 1); }
    for (int c = 2; c < 14; ++c) for (int p = 0; p < 5; ++p) { int pos = 20 * c - 6 + 1 + p * 4; if (c == 8 && pos < 160) pos = 160; s.add(pos, K_W, V_P3 + c + 4, p); }
    for (int i = 0; i < 4; ++i) s.add(159, K_X, X_HST, i);
    s.add(250, K_X, X_FLAG, 0);
    s.sort(); return s;
}
#define P4_AD 6
constexpr Sched<128> make_p4() {
    Sched<128> s;
    for (int i = 0; i < 3; ++i) s.add(-3, K_X, X_HST, i);
    for (int k = 0; k < P4_AD; ++k) { s.add(-2, K_A, k, 0); s.add(-2, K_A, k, 1); }
    for (int c = 0; c < 2; ++c) for (int p = 0; p < 5; ++p) s.add(-1, K_W, V_P4 + 4 + c, p);
    for (int k = P4_AD; k < 27; ++k) { s.add(5 * (k - P4_AD), K_A, k, 0); s.add(5 * (k - P4_AD) + 2, K_A, k, 1); }
    for (int c = 2; c < 10; ++c) for (int p = 0; p < 5; ++p) { const int o[5] = {1, 3, 4, 6, 8}; s.add(10 * c - 4 + o[p], K_W, V_P4 + c + 4, p); }
    s.add(5 * (16 - P4_AD) - 17 > 0 ? 5 * (16 - P4_AD) - 17 : 0, K_X, X_FLAG, 0);
    s.sort(); return s;
}
constexpr auto SP1 = make_p1();
constexpr auto SP3 = make_p3();
template <int PH> constexpr Sched<128> SPR = PH == 2 ? make_p2() : make_p4();

constexpr int L_RING = 0, L_SCR = NSLOT * SLOT, L_IMG = L_SCR + 8 * FRAG, L_TAB = L_SCR + 32 * FRAG, L_CSQ = L_TAB + 1024, L_CSG = L_CSQ + 512, L_STAMP = L_CSG + 1024,
              LDS_BYTES = L_STAMP + 512;
struct MainArgs { char* ws; char* out; };

DI void row_stats(const u32x4& p, float& rstd, float& mrs) {
    float s = __uint_as_float(p[0]) + __uint_as_float(p[2]), s2 = __uint_as_float(p[1]) + __uint_as_float(p[3]);
    s = xg_sum(s); s2 = xg_sum(s2);
    const float mean = s * (1.f / DM), var = fmaxf(s2 * (1.f / DM) - mean * mean, 0.f);
    rstd = __builtin_amdgcn_rsqf(var + 1e-5f);
    mrs = mean * rstd;
}
DI void spin_ge(const char* ws, int line, unsigned target) {
    gu32* p = (gu32*)(ws + WS_CTL + (size_t)line * 256);
    gu32* tmo = (gu32*)(ws + WS_CTL + C_TMO * 256);
    unsigned spins = 0;
    while ((unsigned)__builtin_amdgcn_readfirstlane(__hip_atomic_load(p, __ATOMIC_RELAXED, __HIP_MEMORY_SCOPE_AGENT)) < target) {
        __builtin_amdgcn_s_sleep(1);
        ++spins;
        if ((spins & 1023u) == 0u && __builtin_amdgcn_readfirstlane(__hip_atomic_load(tmo, __ATOMIC_RELAXED, __HIP_MEMORY_SCOPE_AGENT))) break;
        if (spins > (1u << 19)) { __hip_atomic_store(tmo, 1u, __ATOMIC_RELAXED, __HIP_MEMORY_SCOPE_AGENT); break; }
    }
    asm volatile("" ::: "memory");
}
DI void signal(char* ws, int line) { __hip_atomic_fetch_add((gu32*)(ws + WS_CTL + (size_t)line * 256), 1u, __ATOMIC_RELAXED, __HIP_MEMORY_SCOPE_AGENT); }

__global__ __launch_bounds__(256, 1) void main_kernel(MainArgs a) {
    extern __shared__ __attribute__((aligned(16))) char shm[];
    const unsigned lds0 = (unsigned)(size_t)shm;
    LAS char* lds = (LAS char*)shm;
    char* const ws = a.ws;
    const int tid = threadIdx.x, wid = __builtin_amdgcn_readfirstlane(tid >> 6), lane = tid & 63, g = lane >> 4, r = lane & 15;
    const unsigned voff = lane * 16;
    int rp, cg; map16x16(blockIdx.x, rp, cg);
    int rt, c8; map32x8(blockIdx.x, rt, c8);
    const int mh = wid >> 1, kh = wid & 1;
    const int mt_a = rp * 8 + wid * 2;
    const int mt_p = rt * 4 + mh * 2;
    const int mtf = mt_p + kh, odd = c8 & 1;
    const int tA = odd ? (5 * c8 + 1) / 2 : 5 * c8 / 2, tH = odd ? (5 * c8 - 1) / 2 : 5 * c8 / 2 + 2;
    const unsigned hoff = tH * FRAG + (odd * 2 + (g >> 1)) * 256 + r * 16 + (g & 1) * 8;
    STAMP(0);

    const char* wP1 = ws + WS_WQKV + (size_t)(cg * 160 + wid * 5) * FRAG;
    const char* wP2 = ws + WS_WO + (size_t)(c8 * 100 + wid * 5) * FRAG;
    const char* wP3 = ws + WS_WGU + (size_t)(cg * 280 + wid * 5) * FRAG;
    const char* wP4 = ws + WS_WD + (size_t)(c8 * 280 + wid * 5) * FRAG;
    unsigned wdst = lds0 + L_RING + wid * 5 * FRAG;
    auto fresh_ptrs = [&]() { pins(wP1); pins(wP2); pins(wP3); pins(wP4); pins(wdst); };
    auto w_op = [&](auto V, auto P) {
        constexpr int v0 = V, v = v0 % V_END, p = P;
        if constexpr (v != 13) {
            const char* src = v < V_P2 ? wP1 + (size_t)(v - V_P1) * SLOT : v < V_P3 ? wP2 + (size_t)(v - V_P2) * SLOT : v < V_P4 ? wP3 + (size_t)(v - V_P3) * SLOT : wP4 + (size_t)(v - V_P4) * SLOT;
            dma_piece<p>(src, wdst + slot_of(v), voff);
        }
    };
    auto w_chunk = [&](auto V) { sfor<0, 5>([&](auto P) { w_op(V, P); }); };
    const unsigned rdb = lds0 + L_RING + voff, rdb_hi = rdb + 65536;

    u32x4 resA, resB; u32x2 resH;
    {
        u32x4 cq[5], cgp[5];
        const char* csq = ws + WS_CSQ + (size_t)cg * 128 * 80;
        const char* csg = ws + WS_CSG + (size_t)cg * 224 * 80;
        const unsigned oq = (tid & 127) * 80, og = (tid < 224 ? tid : 0) * 80;
        sfor<0, 5>([&](auto J) { ld_v<J * 16>(cq[J], csq, oq); });
        sfor<0, 5>([&](auto J) { ld_v<J * 16>(cgp[J], csg, og); });
        const char* xr = ws + WS_X + (size_t)mtf * KSD * FRAG;
        ld_v<0>(resA, xr + tA * FRAG, voff); ld_v<FRAG>(resB, xr + tA * FRAG, voff); ld_v2(resH, xr, hoff);
        sfor<0, 6>([&](auto V) { w_chunk(V); });
        wait_vm<30>();
        float sq = 0.f, sg = 0.f;
        sfor<0, 5>([&](auto J) { constexpr int j = J; pin(cq[j]); pin(cgp[j]);
            sq += (__uint_as_float(cq[j][0]) + __uint_as_float(cq[j][1])) + (__uint_as_float(cq[j][2]) + __uint_as_float(cq[j][3]));
            sg += (__uint_as_float(cgp[j][0]) + __uint_as_float(cgp[j][1])) + (__uint_as_float(cgp[j][2]) + __uint_as_float(cgp[j][3])); });
        pin(resA); pin(resB); pin(resH);
        if (tid < 128) *(LAS float*)(lds + L_CSQ + tid * 4) = sq;
        if (tid < 224) *(LAS float*)(lds + L_CSG + tid * 4) = sg;
    }

    for (int l = 0; l < 2; ++l) {
        {
            const char* xsrc = ws + WS_X + (size_t)(2 * l) * XT_BYTES + (size_t)mt_a * KSD * FRAG;
            const char* stsrc = ws + WS_ST + (size_t)(2 * l) * ST_BYTES + (size_t)mt_a * 16 * 64;
            char* kvp = ws + WS_KVP + (size_t)((l * 16 + cg) * 16) * KVP_SLOT;
            if (l > 0 && wid == 0) spin_ge(ws, C_X2 + rp, 16u);
            wait_vm<0>(); wait_lgkm<0>(); bar();
            STAMP(l * 24 + 1);
            fresh_ptrs(); pins(xsrc);
            bf16x8 A[2][20];
            f32x4 acc1[2][5], acc2[2][3];
            u32x4 stp[2]; u32x4 kvsum;
            auto emit = [&](auto K) {
                constexpr Op o = SP1.op[K];
                if constexpr (o.kind == K_A) ld_a<(o.a % 4) * FRAG>(A[o.b][o.a], xsrc + (size_t)(o.b * KSD + (o.a / 4) * 4) * FRAG, voff);
                else if constexpr (o.kind == K_W) w_op(IC<o.a>{}, IC<o.b>{});
                else if constexpr (o.a == X_STAT) ld_v<o.b * FRAG>(stp[o.b], stsrc, r * 64 + g * 16);
            };
            sfor<0, SP1.before(0)>(emit);
            constexpr int L = 4, R = 5;
            bf16x8 B[R];
            {
                auto rd = [&](auto I) { constexpr int i = I; if constexpr (i < 100) ds_rd_at<slot_of(V_P1 + i / 20) + (i % 20) * FRAG>(B[i % R], rdb, rdb_hi); };
                sfor<0, L>(rd);
                wait_lgkm<L - 1>();
                sfor<0, 100>([&](auto I) {
                    constexpr int i = I, ks = i / 5, n = i % 5;
                    if constexpr (n == 0) wait_vm<SP1.wait(i, K_A, ks, 1)>();
                    if constexpr (i == 60) bar();
                    mma_t<ks == 0>(acc1[0][n], B[i % R], A[0][ks]);
                    rd(IC<i + L>{});
                    mma_t<ks == 0>(acc1[1][n], B[i % R], A[1][ks]);
                    sfor<SP1.before(i), (i == 99 ? SP1.find(K_X, X_KVST, 0) : SP1.before(i + 1))>(emit);
                    if constexpr (i + 1 < 100) wait_lgkm<cmin(L - 1, 98 - i)>();
                });
            }
            mma_settle();
            sfor<0, 5>([&](auto N) { mma_fence(acc1[0][N]); mma_fence(acc1[1][N]); });
            STAMP(l * 24 + 2);
            float rstd[2], mrs[2];
            pin(stp[0]); pin(stp[1]);
            row_stats(stp[0], rstd[0], mrs[0]); row_stats(stp[1], rstd[1], mrs[1]);
            if (g == 0) {
                *(LAS f32x2*)(lds + L_TAB + (wid * 32 + r) * 8) = (f32x2){rstd[0], mrs[0]};
                *(LAS f32x2*)(lds + L_TAB + (wid * 32 + 16 + r) * 8) = (f32x2){rstd[1], mrs[1]};
            }
            {
                u32x4 op[5];
                f32x4 st[2][2];
                sfor<0, 2>([&](auto M) { st[M][0] = *(LAS f32x4*)(lds + L_TAB + (wid * 32 + M * 16 + 4 * g) * 8); st[M][1] = *(LAS f32x4*)(lds + L_TAB + (wid * 32 + M * 16 + 4 * g) * 8 + 16); });
                sfor<0, 5>([&](auto N) {
                    constexpr int n = N;
                    const float cs = *(LAS float*)(lds + L_CSQ + (n * 16 + r) * 4);
                    float v[2][4];
                    sfor<0, 2>([&](auto M) {
                        sfor<0, 4>([&](auto J) { constexpr int m = M, j = J;
                            float t = acc1[m][n][j] * st[m][j >> 1][(j & 1) * 2] - st[m][j >> 1][(j & 1) * 2 + 1] * cs;
                            if constexpr (n < 2) t = phi(t); else if constexpr (n == 2) t = r < 8 ? phi(t) : t;
                            v[m][j] = t; });
                    });
                    op[n] = (u32x4){pk_bf16(v[0][0], v[0][1]), pk_bf16(v[0][2], v[0][3]), pk_bf16(v[1][0], v[1][1]), pk_bf16(v[1][2], v[1][3])};
                });
                u32x4 v2 = op[2];
                if (r < 8) v2 = (u32x4){0x3f803f80u, 0x3f803f80u, 0x3f803f80u, 0x3f803f80u};
                const f32x4 z4 = {0.f, 0.f, 0.f, 0.f};
                f32x4 kv[8];
                kv[0] = mfma_c(op[0], op[3], z4); kv[1] = mfma_c(op[0], op[4], z4); kv[2] = mfma_c(op[0], v2, z4);
                kv[3] = mfma_c(op[1], op[3], z4); kv[4] = mfma_c(op[1], op[4], z4); kv[5] = mfma_c(op[1], v2, z4);
                kv[6] = mfma_c(op[2], op[4], z4); kv[7] = mfma_c(op[2], v2, z4);
                sfor<0, 8>([&](auto T) { *(LAS f32x4*)(lds + L_SCR + (wid * 8 + T) * FRAG + voff) = kv[T]; });
            }
            wait_lgkm<0>(); bar();
            int s_dg, s_e; bool s_z;
            { const int t = tid < 210 ? tid : 209; s_z = t >= 200; const int hd = t / 100, v = t % 100; s_dg = s_z ? t - 200 : hd * 5 + v / 20; s_e = s_z ? 0 : hd * 20 + v % 20; }
            {
                const int dt = s_dg >> 2, gg = s_dg & 3;
                const int et = s_z ? 2 : s_e >> 4, cc = s_z ? 0 : (s_e < 32 ? s_e & 15 : s_e - 24);
                const int T = dt == 2 ? 5 + et : dt * 3 + et;
                const unsigned o = L_SCR + T * FRAG + (gg * 16 + cc) * 16;
                const f32x4 sum = (*(LAS f32x4*)(lds + o) + *(LAS f32x4*)(lds + o + 8 * FRAG)) + (*(LAS f32x4*)(lds + o + 16 * FRAG) + *(LAS f32x4*)(lds + o + 24 * FRAG));
                kvsum = __builtin_bit_cast(u32x4, sum);
                st_v(kvp + rp * KVP_SLOT, (tid < 210 ? tid : 209) * 16, kvsum);
            }
            STAMP(l * 24 + 3);
            fresh_ptrs();
            {
                constexpr int R2 = 6, L2 = 4;
                bf16x8 B2[R2];
                auto rd = [&](auto I) { constexpr int i = I; if constexpr (i < 60) ds_rd_at<slot_of(V_P1 + 5 + i / 20) + (i % 20) * FRAG>(B2[i % R2], rdb, rdb_hi); };
                sfor<0, L2>(rd);
                wait_lgkm<L2 - 1>();
                sfor<0, 60>([&](auto I) {
                    constexpr int i = I, ks = i / 3, n = i % 3, pos = 100 + i;
                    if constexpr (i == 20 - L2) { wait_vm<SP1.wait(pos, K_W, 6, 4)>(); bar(); }
                    if constexpr (i == 40 - L2) { wait_vm<cmin(SP1.wait(pos, K_W, 7, 4), SP1.wait(pos, K_X, X_KVST, 0))>(); bar(); if (tid == 0) signal(ws, C_KV + l * 16 + cg); }
                    mma<ks == 0>(acc2[0][n], B2[i % R2], A[0][ks]);
                    rd(IC<i + L2>{});
                    mma<ks == 0>(acc2[1][n], B2[i % R2], A[1][ks]);
                    sfor<SP1.before(pos), SP1.before(pos + 1)>(emit);
                    if constexpr (i + 1 < 60) wait_lgkm<cmin(L2 - 1, 58 - i)>();
                });
            }
            mma_settle();
            sfor<0, 3>([&](auto N) { mma_fence(acc2[0][N]); mma_fence(acc2[1][N]); });
            STAMP(l * 24 + 4);
            for (int i = tid; i < 6 * FRAG / 16; i += 256) *(LAS u32x4*)(lds + L_IMG + i * 16) = (u32x4){0u, 0u, 0u, 0u};
            u32x4 qa[2], qb[2];
            sfor<0, 2>([&](auto M) {
                constexpr int m = M;
                unsigned pk[3][2];
                sfor<0, 3>([&](auto N) {
                    constexpr int n = N;
                    const f32x4 cs = *(LAS f32x4*)(lds + L_CSQ + ((5 + n) * 16 + 4 * g) * 4);
                    float v[4];
                    sfor<0, 4>([&](auto J) { constexpr int j = J; v[j] = phi(acc2[m][n][j] * rstd[m] - mrs[m] * cs[j]); });
                    pk[n][0] = pk_bf16(v[0], v[1]); pk[n][1] = pk_bf16(v[2], v[3]);
                });
                qa[m] = (u32x4){pk[0][0], pk[0][1], pk[2][0], pk[2][1]};
                qb[m] = (u32x4){pk[1][0], pk[1][1], pk[2][0], pk[2][1]};
            });
            pin(qa[0]); pin(qa[1]); pin(qb[0]); pin(qb[1]);
            if (wid == 0) spin_ge(ws, C_KV + l * 16 + cg, 16u);
            wait_lgkm<0>(); bar();
            STAMP(l * 24 + 5);
            {
                u32x4 pv[16];
                const unsigned po = (tid < 210 ? tid : 209) * 16;
                sfor<0, 16>([&](auto I) { ld_v<0>(pv[I], kvp + I * KVP_SLOT, po); });
                wait_vm<0>();
                sfor<0, 16>([&](auto I) { pin(pv[I]); });
                f32x4 s = __builtin_bit_cast(f32x4, pv[0]);
#pragma unroll
                for (int i = 1; i < 16; ++i) s += __builtin_bit_cast(f32x4, pv[i]);
                const u32x2 w = {pk_bf16(s[0], s[1]), pk_bf16(s[2], s[3])};
                const int hd = s_dg >= 5, dl = s_dg - hd * 5;
                const int gg = dl < 4 ? dl : hd, hb = dl < 4 ? 0 : 8;
                if (tid < 200) {
                    const int e = s_e, T = e >= 32 ? 2 : (e >> 2) & 1, c = e >= 32 ? e - 32 : (e >> 3) * 4 + (e & 3);
                    const int img = T == 2 ? 4 : T * 2 + hd;
                    *(LAS u32x2*)(lds + L_IMG + img * FRAG + (gg * 16 + c) * 16 + hb) = w;
                } else if (tid < 210) {
                    *(LAS u32x2*)(lds + L_IMG + 5 * FRAG + hd * 64 + gg * 16 + hb) = w;
                }
            }
            wait_lgkm<0>(); bar();
            STAMP(l * 24 + 6);
            {
                u32x4 im[5];
                sfor<0, 5>([&](auto T) { im[T] = *(LAS u32x4*)(lds + L_IMG + T * FRAG + voff); });
                const u32x4 da = *(LAS u32x4*)(lds + L_IMG + 5 * FRAG + g * 16), db = *(LAS u32x4*)(lds + L_IMG + 5 * FRAG + 64 + g * 16);
                const f32x4 z4 = {0.f, 0.f, 0.f, 0.f};
                const char* obase = ws + WS_O + (size_t)l * XT_BYTES + (size_t)mt_a * KSD * FRAG;
                u32x2 t2p[2];
                sfor<0, 2>([&](auto M) {
                    constexpr int m = M;
                    f32x4 t0 = mfma_c(im[0], qa[m], z4); t0 = mfma_c(im[1], qb[m], t0);
                    f32x4 t1 = mfma_c(im[2], qa[m], z4); t1 = mfma_c(im[3], qb[m], t1);
                    const f32x4 t2 = mfma_c(im[4], qb[m], z4);
                    const f32x4 dA = mfma_c(da, qa[m], z4), dB = mfma_c(db, qb[m], z4);
                    const float ia = 1.f / (dA[0] + 1e-6f), ib = 1.f / (dB[0] + 1e-6f);
                    const float i0 = g < 3 ? ia : ib, i1 = g < 2 ? ia : ib;
                    const u32x4 w = {pk_bf16(t0[0] * i0, t0[1] * i0), pk_bf16(t0[2] * i0, t0[3] * i0), pk_bf16(t1[0] * i1, t1[1] * i1), pk_bf16(t1[2] * i1, t1[3] * i1)};
                    const int G = 5 * cg + g;
                    st_v(obase, (m * KSD + (G >> 2)) * FRAG + (G & 3) * 256 + r * 16, w);
                    t2p[m] = (u32x2){pk_bf16(t2[0] * ib, t2[1] * ib), pk_bf16(t2[2] * ib, t2[3] * ib)};
                });
                const auto s0 = __builtin_amdgcn_permlane16_swap(t2p[0][0], t2p[1][0], false, false);
                const auto s1 = __builtin_amdgcn_permlane16_swap(t2p[0][1], t2p[1][1], false, false);
                const u32x4 w = {s0[0], s1[0], s0[1], s1[1]};
                const int G = 5 * cg + 4;
                if (g < 2) st_v(obase, (g * KSD + (G >> 2)) * FRAG + (G & 3) * 256 + r * 16, w);
            }
            wait_vm<0>(); bar();
            if (tid == 0) signal(ws, C_O + l * 16 + rp);
            fresh_ptrs();
            w_chunk(IC<12>{});
            STAMP(l * 24 + 7);
        }
        auto proj = [&](auto PH) {
            constexpr int ph = PH;
            constexpr int NS = ph == 2 ? 10 : 27, KS = 2 * NS, NF = NS * 5, V0 = ph == 2 ? V_P2 : V_P4, NCH = (NS + 1) / 2;
            const char* asrc = ws + (ph == 2 ? WS_O + (size_t)l * XT_BYTES : WS_H + (size_t)l * HT_BYTES) + (size_t)(mt_p * KS + kh * NS) * FRAG;
            fresh_ptrs(); pins(asrc);
            constexpr int AW = (ph == 4 ? P4_AD : 6) + 2;
            bf16x8 A[AW][2];
            f32x4 acc[2][5];
            unsigned flagv;
            auto emit = [&](auto K) {
                constexpr Op o = SPR<ph>.op[K];
                if constexpr (o.kind == K_A) ld_a<(o.a % 4) * FRAG>(A[o.a % AW][o.b], asrc + (size_t)(o.b * KS + (o.a / 4) * 4) * FRAG, voff);
                else if constexpr (o.kind == K_W) w_op(IC<o.a>{}, IC<o.b>{});
                else if constexpr (o.a == X_FLAG) ld_v1(flagv, ws + WS_CTL + (size_t)(C_HB + l * 16 + (rt >> 1)) * 256, 0u);
            };
            constexpr int first = ph == 2 ? 0 : 3;
            sfor<first, SPR<ph>.before(0)>(emit);
            constexpr int L = 4, R = 5;
            bf16x8 B[R];
            const unsigned rdk = rdb + kh * 5 * FRAG, rdk_hi = rdk + 65536;
            auto rd = [&](auto I) { constexpr int i = I; if constexpr (i < NF) { constexpr int s = i / 5, c = s / 2;
                ds_rd_at<slot_of(V0 + c) + ((s & 1) * 10 + i % 5) * FRAG>(B[i % R], rdk, rdk_hi); } };
            sfor<0, L>(rd);
            wait_lgkm<L - 1>();
            sfor<0, NF>([&](auto I) {
                constexpr int i = I, s = i / 5, n = i % 5, c = s / 2;
                if constexpr (n == 0) wait_vm<SPR<ph>.wait(i, K_A, s, 1)>();
                if constexpr (i % 10 == 10 - L && c + 1 < NCH && c + 1 >= 2) {
                    constexpr int wv = SPR<ph>.wait(i, K_W, V0 + c + 1, 4);
                    if constexpr (ph == 4 && c + 1 == 2) { wait_vm<cmin(wv, SPR<ph>.wait(i, K_X, X_HST, 2))>(); bar(); if (tid == 0) signal(ws, C_HB + l * 16 + rp); }
                    else { wait_vm<wv>(); bar(); }
                }
                if constexpr (ph == 4 && i == 5 * (16 - P4_AD)) {
                    wait_vm<SPR<ph>.wait(i, K_X, X_FLAG, 0)>();
                    pin(flagv);
                    if (__builtin_amdgcn_readfirstlane(flagv) < 16) spin_ge(ws, C_HB + l * 16 + (rt >> 1), 16u);
                }
                mma<s == 0>(acc[0][n], B[i % R], A[s % AW][0]);
                rd(IC<i + L>{});
                mma<s == 0>(acc[1][n], B[i % R], A[s % AW][1]);
                sfor<SPR<ph>.before(i), SPR<ph>.before(i + 1)>(emit);
                if constexpr (i + 1 < NF) wait_lgkm<cmin(L - 1, NF - 2 - i)>();
            });
            mma_settle();
            sfor<0, 5>([&](auto N) { mma_fence(acc[0][N]); mma_fence(acc[1][N]); });
            STAMP(l * 24 + (ph == 2 ? 9 : 17));
            f32x4 fin[5];
            sfor<0, 5>([&](auto N) {
                const f32x4 give = kh ? acc[0][N] : acc[1][N];
                *(LAS f32x4*)(lds + L_SCR + (wid * 5 + N) * FRAG + voff) = give;
            });
            wait_lgkm<0>(); bar();
            sfor<0, 5>([&](auto N) {
                const f32x4 keep = kh ? acc[1][N] : acc[0][N];
                fin[N] = keep + *(LAS f32x4*)(lds + L_SCR + ((wid ^ 1) * 5 + N) * FRAG + voff);
            });
            auto fin_pair = [&](const f32x4& lo, const f32x4& hi, const u32x4& x) {
                u32x4 w;
                w[0] = pk_bf16(lo[0] + bf_lo(x[0]), lo[1] + bf_hi(x[0])); w[1] = pk_bf16(lo[2] + bf_lo(x[1]), lo[3] + bf_hi(x[1]));
                w[2] = pk_bf16(hi[0] + bf_lo(x[2]), hi[1] + bf_hi(x[2])); w[3] = pk_bf16(hi[2] + bf_lo(x[3]), hi[3] + bf_hi(x[3]));
                return w;
            };
            resA = fin_pair(fin[0], fin[1], resA); resB = fin_pair(fin[2], fin[3], resB);
            resH = (u32x2){pk_bf16(fin[4][0] + bf_lo(resH[0]), fin[4][1] + bf_hi(resH[0])), pk_bf16(fin[4][2] + bf_lo(resH[1]), fin[4][3] + bf_hi(resH[1]))};
            if (ph == 4 && l == 1) {
                char* row = a.out + (size_t)(mtf * 16 + r) * 1280;
                *(GAS u32x4*)(row + tA * 64 + g * 16) = resA;
                *(GAS u32x4*)(row + tA * 64 + 64 + g * 16) = resB;
                *(GAS u32x2*)(row + tH * 64 + odd * 32 + g * 8) = resH;
            } else {
                const int xo = 2 * l + (ph == 2 ? 1 : 2);
                const char* xb = ws + WS_X + (size_t)xo * XT_BYTES + (size_t)mtf * KSD * FRAG;
                st_v(xb + tA * FRAG, voff, resA); st_v(xb + (tA + 1) * FRAG, voff, resB); st_v2(xb, hoff, resH);
                float s1 = 0.f, s2 = 0.f;
                auto accum = [&](unsigned u) { const float lo = bf_lo(u), hi = bf_hi(u); s1 += lo + hi; s2 += lo * lo + hi * hi; };
                sfor<0, 4>([&](auto J) { constexpr int j = J; accum(resA[j]); accum(resB[j]); });
                accum(resH[0]); accum(resH[1]);
                s1 = xg_sum(s1); s2 = xg_sum(s2);
                if (g == 0) st_v2(ws + WS_ST + (size_t)xo * ST_BYTES + (size_t)mtf * 16 * 64, r * 64 + c8 * 8, (u32x2){__float_as_uint(s1), __float_as_uint(s2)});
                wait_vm<0>(); bar();
                if (tid == 0) signal(ws, (ph == 2 ? C_X1 + l * 16 : C_X2) + (rt >> 1));
                fresh_ptrs();
                if constexpr (ph == 2) { w_chunk(IC<17>{}); w_chunk(IC<18>{}); w_chunk(IC<19>{}); }
                else sfor<0, 6>([&](auto V) { w_chunk(IC<V_END + V>{}); });
            }
        };
        if (wid == 0) spin_ge(ws, C_O + l * 16 + (rt >> 1), 16u);
        wait_vm<0>(); bar();
        STAMP(l * 24 + 8);
        proj(IC<2>{});
        STAMP(l * 24 + 10);
        {
            const char* xsrc = ws + WS_X + (size_t)(2 * l + 1) * XT_BYTES + (size_t)mt_a * KSD * FRAG;
            const char* stsrc = ws + WS_ST + (size_t)(2 * l + 1) * ST_BYTES + (size_t)mt_a * 16 * 64;
            const char* hb = ws + WS_H + (size_t)l * HT_BYTES + (size_t)mt_a * KSF * FRAG;
            if (wid == 0) spin_ge(ws, C_X1 + l * 16 + rp, 16u);
            wait_vm<0>(); bar();
            STAMP(l * 24 + 11);
            fresh_ptrs(); pins(xsrc);
            bf16x8 A[2][20];
            f32x4 accA[2][8], accB[2][6];
            u32x4 stp[2]; unsigned flagv;
            auto emit = [&](auto K) {
                constexpr Op o = SP3.op[K];
                if constexpr (o.kind == K_A) ld_a<(o.a % 4) * FRAG>(A[o.b][o.a], xsrc + (size_t)(o.b * KSD + (o.a / 4) * 4) * FRAG, voff);
                else if constexpr (o.kind == K_W) w_op(IC<o.a>{}, IC<o.b>{});
                else if constexpr (o.a == X_STAT) ld_v<o.b * FRAG>(stp[o.b], stsrc, r * 64 + g * 16);
                else if constexpr (o.a == X_FLAG) ld_v1(flagv, ws + WS_CTL + (size_t)(C_HA + l * 16 + (rt >> 1)) * 256, 0u);
            };
            sfor<0, SP3.before(0)>(emit);
            constexpr int L = 6, R = 7;
            bf16x8 B[R];
            auto rd = [&](auto I, auto LIM) { constexpr int i = I; if constexpr (i < LIM) ds_rd_at<slot_of(V_P3 + i / 20) + (i % 20) * FRAG>(B[i % R], rdb, rdb_hi); };
            sfor<0, L>([&](auto I) { rd(I, IC<160>{}); });
            wait_lgkm<L - 1>();
            sfor<0, 160>([&](auto I) {
                constexpr int i = I, ks = i / 8, n = i % 8;
                if constexpr (n == 0) wait_vm<SP3.wait(i, K_A, ks, 1)>();
                if constexpr (i % 20 == 20 - L && i / 20 + 1 >= 2 && i / 20 + 1 < 8) { wait_vm<SP3.wait(i, K_W, V_P3 + i / 20 + 1, 4)>(); bar(); }
                mma<ks == 0>(accA[0][n], B[i % R], A[0][ks]);
                rd(IC<i + L>{}, IC<160>{});
                mma<ks == 0>(accA[1][n], B[i % R], A[1][ks]);
                sfor<SP3.before(i), (i == 159 ? SP3.find(K_X, X_HST, 0) : SP3.before(i + 1))>(emit);
                if constexpr (i + 1 < 160) wait_lgkm<cmin(L - 1, 158 - i)>();
            });
            mma_settle();
            sfor<0, 8>([&](auto N) { mma_fence(accA[0][N]); mma_fence(accA[1][N]); });
            STAMP(l * 24 + 12);
            float rstd[2], mrs[2];
            pin(stp[0]); pin(stp[1]);
            row_stats(stp[0], rstd[0], mrs[0]); row_stats(stp[1], rstd[1], mrs[1]);
            auto h_tile = [&](const f32x4& ga, const f32x4& ua, int tile, int m) {
                const f32x4 cgv = *(LAS f32x4*)(lds + L_CSG + ((2 * tile) * 16 + 4 * g) * 4), cuv = *(LAS f32x4*)(lds + L_CSG + ((2 * tile + 1) * 16 + 4 * g) * 4);
                const f32x4 h = (ga * rstd[m] - cgv * mrs[m]) * (ua * rstd[m] - cuv * mrs[m]);
                return (u32x2){pk_bf16(h[0], h[1]), pk_bf16(h[2], h[3])};
            };
            sfor<0, 2>([&](auto P) {
                sfor<0, 2>([&](auto M) {
                    constexpr int p = P, m = M;
                    const u32x2 lo = h_tile(accA[m][4 * p], accA[m][4 * p + 1], 2 * p, m), hi = h_tile(accA[m][4 * p + 2], accA[m][4 * p + 3], 2 * p + 1, m);
                    st_v(hb + (size_t)(m * KSF + h_pos_a(cg, p)) * FRAG, voff, (u32x4){lo[0], lo[1], hi[0], hi[1]});
                });
            });
            STAMP(l * 24 + 13);
            fresh_ptrs();
            wait_vm<SP3.wait(160, K_W, V_P3 + 8, 4)>(); wait_lgkm<0>(); bar();
            sfor<160, 160 + L>([&](auto I) { rd(I, IC<280>{}); });
            wait_lgkm<L - 1>();
            sfor<160, 280>([&](auto I) {
                constexpr int i = I, ks = (i - 160) / 6, n = (i - 160) % 6;
                if constexpr (i % 20 == 20 - L && i / 20 + 1 < 14) {
                    constexpr int wv = SP3.wait(i, K_W, V_P3 + i / 20 + 1, 4);
                    if constexpr (i / 20 + 1 == 10) { wait_vm<cmin(wv, SP3.wait(i, K_X, X_HST, 3))>(); bar(); if (tid == 0) signal(ws, C_HA + l * 16 + rp); }
                    else { wait_vm<wv>(); bar(); }
                }
                mma<ks == 0>(accB[0][n], B[i % R], A[0][ks]);
                rd(IC<i + L>{}, IC<280>{});
                mma<ks == 0>(accB[1][n], B[i % R], A[1][ks]);
                sfor<SP3.before(i), SP3.before(i + 1)>(emit);
                if constexpr (i + 1 < 280) wait_lgkm<cmin(L - 1, 278 - i)>();
            });
            mma_settle();
            sfor<0, 6>([&](auto N) { mma_fence(accB[0][N]); mma_fence(accB[1][N]); });
            STAMP(l * 24 + 14);
            wait_vm<SP3.n - SP3.find(K_X, X_FLAG, 0) - 1>();
            pin(flagv);
            u32x2 t6[2];
            sfor<0, 2>([&](auto M) {
                constexpr int m = M;
                const u32x2 lo = h_tile(accB[m][0], accB[m][1], 4, m), hi = h_tile(accB[m][2], accB[m][3], 5, m);
                st_v(hb + (size_t)(m * KSF + h_pos_b(cg)) * FRAG, voff, (u32x4){lo[0], lo[1], hi[0], hi[1]});
                t6[m] = h_tile(accB[m][4], accB[m][5], 6, m);
            });
            {
                const auto s0 = __builtin_amdgcn_permlane16_swap(t6[0][0], t6[1][0], false, false);
                const auto s1 = __builtin_amdgcn_permlane16_swap(t6[0][1], t6[1][1], false, false);
                const u32x4 w = {s0[0], s1[0], s0[1], s1[1]};
                const char* dst = cg < 12 ? hb + (size_t)h_pos_b(16 + (cg >> 1)) * FRAG : ws + WS_DUMMY;
                const unsigned off = cg < 12 ? (g & 1) * KSF * FRAG + (2 * (cg & 1) + (g >> 1)) * 256 + r * 16 : (blockIdx.x * 4 + wid) * FRAG + voff;
                st_v(dst, off, w);
            }
            if (__builtin_amdgcn_readfirstlane(flagv) < 16) spin_ge(ws, C_HA + l * 16 + (rt >> 1), 16u);
            bar();
            STAMP(l * 24 + 15);
        }
        proj(IC<4>{});
        STAMP(l * 24 + 18);
    }
}

extern "C" void kernel_launch(void* const* d_in, const int* in_sizes, int n_in, void* d_out, int out_size, void* d_ws, size_t ws_size, hipStream_t stream) {
    char* ws = (char*)d_ws;
    (void)hipFuncSetAttribute((const void*)prep_kernel, hipFuncAttributeMaxDynamicSharedMemorySize, 4 * PREP_WAVE_LDS);
    (void)hipFuncSetAttribute((const void*)main_kernel, hipFuncAttributeMaxDynamicSharedMemorySize, LDS_BYTES);
    PrepArgs pa{(const char*)d_in[0], (const char*)d_in[1], (const char*)d_in[2], (const char*)d_in[4], (const char*)d_in[5], (const char*)d_in[6], ws};
    hipLaunchKernelGGL(prep_kernel, dim3(PREP_UNITS / 4), dim3(256), 4 * PREP_WAVE_LDS, stream, pa);
    MainArgs ma{ws, (char*)d_out};
    hipLaunchKernelGGL(main_kernel, dim3(256), dim3(256), LDS_BYTES, stream, ma);
}
```

```cpp
#include <hip/hip_runtime.h>
#include <hip/hip_bf16.h>
#include <type_traits>

#define LAS __attribute__((address_space(3)))
#define GAS __attribute__((address_space(1)))
#define DI __device__ __forceinline__
typedef short bf16x8 __attribute__((ext_vector_type(8)));
typedef short s16x4 __attribute__((ext_vector_type(4)));
typedef float f32x4 __attribute__((ext_vector_type(4)));
typedef float f32x2 __attribute__((ext_vector_type(2)));
typedef unsigned u32x4 __attribute__((ext_vector_type(4)));
typedef unsigned u32x2 __attribute__((ext_vector_type(2)));
typedef __bf16 bf16x2_n __attribute__((ext_vector_type(2)));
typedef GAS unsigned gu32;

constexpr int SEQ = 2048, DM = 640;
constexpr int KSD = 20, KSF = 56, NMT = 128;
constexpr int FRAG = 1024;

constexpr size_t WS_CTL = 0;
constexpr int CTL_LINES = 256;
constexpr int C_TMO = 0, C_KV = 1, C_O = 33, C_X1 = 65, C_HA = 97, C_HB = 129, C_X2 = 161;
constexpr size_t CTL_BYTES = (size_t)CTL_LINES * 256;
constexpr size_t WS_WQKV = CTL_BYTES;
constexpr size_t WS_WO = WS_WQKV + (size_t)16 * 160 * FRAG;
constexpr size_t WS_WGU = WS_WO + (size_t)8 * 100 * FRAG;
constexpr size_t WS_WD = WS_WGU + (size_t)16 * 280 * FRAG;
constexpr size_t WS_CSQ = WS_WD + (size_t)8 * 280 * FRAG;
constexpr size_t WS_CSG = WS_CSQ + (size_t)16 * 128 * 20 * 4;
constexpr size_t XT_BYTES = (size_t)NMT * KSD * FRAG;
constexpr size_t WS_X = WS_CSG + (size_t)16 * 224 * 20 * 4;
constexpr size_t ST_BYTES = (size_t)SEQ * 64;
constexpr size_t WS_ST = WS_X + 4 * XT_BYTES;
constexpr size_t WS_O = WS_ST + 4 * ST_BYTES;
constexpr size_t HT_BYTES = (size_t)NMT * KSF * FRAG;
constexpr size_t WS_H = WS_O + 2 * XT_BYTES;
constexpr size_t KVP_SLOT = 3584;
constexpr size_t WS_KVP = WS_H + 2 * HT_BYTES;
constexpr size_t WS_DUMMY = WS_KVP + (size_t)2 * 16 * 16 * KVP_SLOT;
constexpr size_t WS_END = WS_DUMMY + (size_t)1024 * FRAG;

template <int I, int N, class F> DI void sfor(F&& f) { if constexpr (I < N) { f(std::integral_constant<int, I>{}); sfor<I + 1, N>(f); } }
template <int V> using IC = std::integral_constant<int, V>;

DI unsigned pk_bf16(float lo, float hi) { const bf16x2_n v = __builtin_convertvector((f32x2){lo, hi}, bf16x2_n); return __builtin_bit_cast(unsigned, v); }
DI float bf_lo(unsigned u) { return __uint_as_float(u << 16); }
DI float bf_hi(unsigned u) { return __uint_as_float(u & 0xffff0000u); }
DI float xg_sum(float v) {
    const unsigned u = __float_as_uint(v);
    const auto a = __builtin_amdgcn_permlane16_swap(u, u, false, false);
    const float s = __uint_as_float(a[0]) + __uint_as_float(a[1]);
    const unsigned w = __float_as_uint(s);
    const auto b = __builtin_amdgcn_permlane32_swap(w, w, false, false);
    return __uint_as_float(b[0]) + __uint_as_float(b[1]);
}

template <int N> DI void wait_vm() { static_assert(N >= 0 && N < 64, "vmcnt range"); asm volatile("s_waitcnt vmcnt(%0)" ::"i"(N) : "memory"); }
template <int N> DI void wait_lgkm() { static_assert(N >= 0 && N < 16, "lgkmcnt range"); asm volatile("s_waitcnt lgkmcnt(%0)" ::"i"(N) : "memory"); }
DI void bar() { asm volatile("s_barrier" ::: "memory"); }
template <int OFF> DI void ds_rd(bf16x8& d, unsigned addr) { asm volatile("ds_read_b128 %0, %1 offset:%2" : "=v"(d) : "v"(addr), "i"(OFF) : "memory"); }
template <int T> DI void ds_rd_at(bf16x8& d, unsigned lo, unsigned hi) { ds_rd<T % 65536>(d, T < 65536 ? lo : hi); }
template <int OFF> DI void ld_a(bf16x8& d, const char* sb, unsigned voff) { asm volatile("global_load_dwordx4 %0, %1, %2 offset:%3 sc1" : "=a"(d) : "v"(voff), "s"(sb), "i"(OFF) : "memory"); }
template <int OFF> DI void ld_v(u32x4& d, const char* sb, unsigned voff) { asm volatile("global_load_dwordx4 %0, %1, %2 offset:%3 sc1" : "=v"(d) : "v"(voff), "s"(sb), "i"(OFF) : "memory"); }
DI void ld_v2(u32x2& d, const char* sb, unsigned voff) { asm volatile("global_load_dwordx2 %0, %1, %2 sc1" : "=v"(d) : "v"(voff), "s"(sb) : "memory"); }
template <int OFF = 0> DI void ld_v1(unsigned& d, const char* sb, unsigned voff) { asm volatile("global_load_dword %0, %1, %2 offset:%3 sc1" : "=v"(d) : "v"(voff), "s"(sb), "i"(OFF) : "memory"); }
DI void st_v(const char* sb, unsigned voff, const u32x4& v) { asm volatile("global_store_dwordx4 %0, %1, %2 sc1\n\ts_nop 1" ::"v"(voff), "v"(v), "s"(sb) : "memory"); }
DI void st_v2(const char* sb, unsigned voff, const u32x2& v) { asm volatile("global_store_dwordx2 %0, %1, %2 sc1\n\ts_nop 1" ::"v"(voff), "v"(v), "s"(sb) : "memory"); }
DI void set_m0(unsigned lds) { asm volatile("s_mov_b32 m0, %0\n\ts_nop 0" ::"s"(lds) : "memory"); }
template <int OFF> DI void dma(const char* sb, unsigned voff) { asm volatile("global_load_lds_dwordx4 %0, %1 offset:%2" ::"v"(voff), "s"(sb), "i"(OFF) : "memory"); }
template <int P> DI void dma_piece(const char* sb, unsigned lds, unsigned voff) {
    if constexpr (P % 4 == 0) { set_m0(lds + P * FRAG); }
    dma<(P % 4) * FRAG>(sb + (P / 4) * 4 * FRAG, voff);
}
template <bool FIRST> DI void mma(f32x4& c, const bf16x8& w, const bf16x8& x) {
    if constexpr (FIRST) asm volatile("v_mfma_f32_16x16x32_bf16 %0, %1, %2, 0" : "=v"(c) : "v"(w), "a"(x));
    else asm volatile("v_mfma_f32_16x16x32_bf16 %0, %1, %2, %0" : "+v"(c) : "v"(w), "a"(x));
}
template <bool FIRST> DI void mma_t(f32x4& c, const bf16x8& w, const bf16x8& x) {
    if constexpr (FIRST) asm volatile("v_mfma_f32_16x16x32_bf16 %0, %2, %1, 0" : "=v"(c) : "v"(w), "a"(x));
    else asm volatile("v_mfma_f32_16x16x32_bf16 %0, %2, %1, %0" : "+v"(c) : "v"(w), "a"(x));
}
DI void mma_settle() { asm volatile("s_nop 7\n\ts_nop 7" ::: "memory"); }
DI void mma_fence(f32x4& c) { asm volatile("" : "+v"(c)); }
DI void undef_a(bf16x8& x) { asm volatile("" : "=a"(x)); }
template <class T> DI void pins(T& x) { asm volatile("" : "+s"(x)); }
template <class T> DI void pin(T& x) { asm volatile("" : "+v"(x)); }

DI f32x4 mfma_c(const u32x4& a, const u32x4& b, const f32x4& c) {
    return __builtin_amdgcn_mfma_f32_16x16x32_bf16(__builtin_bit_cast(bf16x8, a), __builtin_bit_cast(bf16x8, b), c, 0, 0, 0);
}
DI float phi(float t) { return (t > 0.f ? t + 1.f : __expf(t)) + 1e-6f; }

__host__ __device__ constexpr int h_tile_base(int f) { return f <= 12 ? 7 * f : 84 + 6 * (f - 12); }
__host__ __device__ constexpr int h_pos_a(int f, int p) { return ((2 * f + p) >> 3) * 14 + ((2 * f + p) & 7); }
__host__ __device__ constexpr int h_pos_b(int ib) { return ib < 12 ? (ib / 6) * 14 + 8 + ib % 6 : (2 + (ib - 12) / 5) * 14 + 8 + (ib - 12) % 5; }

#define STAMP(i)

DI void map16x16(int b, int& r, int& c) { const int x = b & 7, q = b >> 3; r = (x >> 2) * 8 + (q & 7); c = (x & 3) * 4 + (q >> 3); }
DI void map32x8(int b, int& r, int& c) { const int x = b & 7, q = b >> 3; r = (x >> 1) * 8 + (q & 7); c = (x & 1) * 4 + (q >> 3); }

constexpr int PU_X = 32, PU_Q = 40, PU_G = 80, PU_O = 20, PU_D = 54, PREP_UNITS = PU_X + PU_Q + PU_G + PU_O + PU_D;
constexpr int PREP_LDS = 60 * 1024;
struct PrepArgs { const char *x, *wqkv, *wo, *wg, *wu, *wd; char* ws; };

template <int N> DI void tr_frags(bf16x8 (&fr)[N], const unsigned (&addr)[N], unsigned rs) {
    s16x4 lo[N], hi[N];
#pragma unroll
    for (int i = 0; i < N; ++i) asm volatile("ds_read_b64_tr_b16 %0, %2\n\tds_read_b64_tr_b16 %1, %3" : "=&v"(lo[i]), "=&v"(hi[i]) : "v"(addr[i]), "v"(addr[i] + 4 * rs) : "memory");
    asm volatile("s_waitcnt lgkmcnt(0)" ::: "memory");
#pragma unroll
    for (int i = 0; i < N; ++i) { asm volatile("" : "+v"(lo[i]), "+v"(hi[i])); fr[i] = __builtin_shufflevector(lo[i], hi[i], 0, 1, 2, 3, 4, 5, 6, 7); }
}
DI void prep_st16(char* dst, const u32x4& v) { asm volatile("global_store_dwordx4 %0, %1, off sc1\n\ts_nop 1" ::"v"(dst), "v"(v) : "memory"); }
DI void prep_store(char* dst, const bf16x8& fr, int lane) { prep_st16(dst + lane * 16, __builtin_bit_cast(u32x4, fr)); }
DI void prep_colsum(float* dst, const bf16x8& fr, int lane) {
    const bf16x8 ones = {0x3f80, 0x3f80, 0x3f80, 0x3f80, 0x3f80, 0x3f80, 0x3f80, 0x3f80};
    const f32x4 z = {0.f, 0.f, 0.f, 0.f};
    const f32x4 d = __builtin_amdgcn_mfma_f32_16x16x32_bf16(fr, ones, z, 0, 0, 0);
    if ((lane & 15) == 0) prep_st16((char*)(dst + (lane >> 4) * 4), __builtin_bit_cast(u32x4, d));
}
DI void prep_dma(const char* src, LAS char* dst) { __builtin_amdgcn_global_load_lds((const GAS unsigned*)src, (LAS unsigned*)dst, 16, 0, 0); }

__global__ __launch_bounds__(256, 2) void prep_kernel(PrepArgs a) {
    extern __shared__ __attribute__((aligned(16))) char shm[];
    const int tid = threadIdx.x, wid = __builtin_amdgcn_readfirstlane(tid >> 6), lane = tid & 63;
    int u = blockIdx.x;
    const int g = lane >> 4, q = (lane & 15) >> 2, p = lane & 3, c16 = lane & 15;
    LAS char* img = (LAS char*)shm;
    const unsigned imgaddr = (unsigned)(size_t)shm;
    const bf16x8 zero = {0, 0, 0, 0, 0, 0, 0, 0};
#define PSTAMP(x)
#define POUT()
    if (u < PU_Q) {
        const int ks = u >> 1, hh = u & 1;
#pragma unroll
        for (int it = 0; it < 15; ++it) {
            const int i = (it * 4 + wid) * 64 + lane, row = i / 120, cc = i % 120, seg = cc / 40, c = cc % 40;
            prep_dma(a.wqkv + (size_t)(ks * 32 + row) * 3840 + seg * 1280 + hh * 640 + c * 16, img + (it * 4 + wid) * 1024);
        }
        wait_vm<0>(); __syncthreads(); PSTAMP(pt1);
        const unsigned rs = 1920;
#pragma unroll
        for (int e = 0; e < 2; ++e) {
            const int hl = wid * 2 + e, hp = hh * 8 + hl;
            const unsigned qa = imgaddr + (8 * g + q) * rs + hl * 80, ka = qa + 640, va = qa + 1280;
            const unsigned ad[8] = {ka + 8 * p, ka + 32 + 8 * p, p < 2 ? ka + 64 + 8 * p : va + 64 + 8 * (p - 2), va + 8 * p, va + 32 + 8 * p, qa + 8 * p, qa + 40 + 8 * p, qa + (p == 0 ? 32 : 72)};
            bf16x8 fr[8];
            tr_frags<8>(fr, ad, rs);
            if (c16 >= 8) fr[7] = zero;
            char* w1 = a.ws + WS_WQKV + (size_t)hp * 160 * FRAG + (size_t)ks * 5 * FRAG;
            char* w2 = a.ws + WS_WQKV + (size_t)hp * 160 * FRAG + (size_t)(100 + ks * 3) * FRAG;
            float* cs = (float*)(a.ws + WS_CSQ) + (hp * 20 + ks) * 128;
#pragma unroll
            for (int nt = 0; nt < 8; ++nt) { prep_store(nt < 5 ? w1 + nt * FRAG : w2 + (nt - 5) * FRAG, fr[nt], lane); prep_colsum(cs + nt * 16, fr[nt], lane); }
        }
        PSTAMP(pt2); POUT();
        return;
    }
    u -= PU_Q;
    if (u < PU_X) {
        if (u == 0) __hip_atomic_store((gu32*)(a.ws + WS_CTL + tid * 256), 0u, __ATOMIC_RELAXED, __HIP_MEMORY_SCOPE_AGENT);
        const int mt = u * 4 + wid, r = lane & 15;
        const char* src = a.x + (size_t)(mt * 16 + r) * 1280 + g * 16;
        char* dst = a.ws + WS_X + (size_t)mt * KSD * FRAG + lane * 16;
        u32x4 v[20];
#pragma unroll
        for (int ks = 0; ks < 20; ++ks) v[ks] = *(const GAS u32x4*)(src + ks * 64);
        const bf16x8 ones = {0x3f80, 0x3f80, 0x3f80, 0x3f80, 0x3f80, 0x3f80, 0x3f80, 0x3f80};
        f32x4 s1 = {0.f, 0.f, 0.f, 0.f}, s2 = {0.f, 0.f, 0.f, 0.f};
#pragma unroll
        for (int ks = 0; ks < 20; ++ks) {
            prep_st16(dst + ks * FRAG, v[ks]);
            const bf16x8 xf = __builtin_bit_cast(bf16x8, v[ks]);
            s1 = __builtin_amdgcn_mfma_f32_16x16x32_bf16(xf, ones, s1, 0, 0, 0);
            s2 = __builtin_amdgcn_mfma_f32_16x16x32_bf16(xf, xf, s2, 0, 0, 0);
        }
        const int j = r & 3;
        const float a1 = j == 0 ? s1[0] : j == 1 ? s1[1] : j == 2 ? s1[2] : s1[3], a2 = j == 0 ? s2[0] : j == 1 ? s2[1] : j == 2 ? s2[2] : s2[3];
        const bool own = (r >> 2) == g;
        const int chunk = (g - (r >> 2)) & 3;
        prep_st16(a.ws + WS_ST + (size_t)(mt * 16 + r) * 64 + chunk * 16, own ? (u32x4){__float_as_uint(a1), __float_as_uint(a2), 0u, 0u} : (u32x4){0u, 0u, 0u, 0u});
        PSTAMP(pt1); PSTAMP(pt2); POUT();
        return;
    }
    u -= PU_X;
    if (u < PU_G) {
        const int ks = u >> 2, fq = u & 3;
#pragma unroll
        for (int it = 0; it < 14; ++it) {
            const int i = (it * 4 + wid) * 64 + lane, row = i / 112, cc = i % 112, up = cc / 56; int c = cc % 56;
            if (fq == 3 && c >= 48) c = 0;
            prep_dma((up ? a.wu : a.wg) + (size_t)(ks * 32 + row) * 3456 + fq * 896 + c * 16, img + (it * 4 + wid) * 1024);
        }
        wait_vm<0>(); __syncthreads(); PSTAMP(pt1);
        const int f = fq * 4 + wid, nt7 = f < 12;
        const unsigned rs = 1792, rowaddr = imgaddr + (8 * g + q) * rs + (h_tile_base(f) - h_tile_base(fq * 4)) * 32;
        unsigned ad[14];
#pragma unroll
        for (int nt = 0; nt < 14; ++nt) { const int j = nt >> 1, up = nt & 1; ad[nt] = rowaddr + up * 896 + 2 * (j < 6 ? 32 * (j >> 1) + 8 * p + 4 * (j & 1) : (nt7 ? 96 : 0) + 4 * p); }
        bf16x8 fr[14];
        tr_frags<14>(fr, ad, rs);
        if (!nt7) { fr[12] = zero; fr[13] = zero; }
        char* da = a.ws + WS_WGU + (size_t)f * 280 * FRAG + (size_t)ks * 8 * FRAG;
        char* db = a.ws + WS_WGU + (size_t)f * 280 * FRAG + (size_t)(160 + ks * 6) * FRAG;
        float* cs = (float*)(a.ws + WS_CSG) + (f * 20 + ks) * 224;
#pragma unroll
        for (int nt = 0; nt < 14; ++nt) { prep_store(nt < 8 ? da + nt * FRAG : db + (nt - 8) * FRAG, fr[nt], lane); prep_colsum(cs + nt * 16, fr[nt], lane); }
        PSTAMP(pt2); POUT();
        return;
    }
    u -= PU_G;
    {
        const bool down = u >= PU_O;
        const int v = down ? u - PU_O : u;
        int kq, s, row_lo, row_hi;
        if (!down) { kq = v / 5; s = v % 5; row_lo = v * 32; row_hi = row_lo + 16; }
        else if (v < 32) { kq = v >> 3; s = v & 7; row_lo = 16 * (h_tile_base(v >> 1) + 2 * (v & 1)); row_hi = row_lo + 16; }
        else {
            const int ib = v - 32, pos = h_pos_b(ib); kq = pos / 14; s = pos % 14;
            if (ib < 16) { row_lo = 16 * (h_tile_base(ib) + 4); row_hi = row_lo + 16; }
            else { const int f0 = 2 * (ib - 16); row_lo = 16 * (h_tile_base(f0) + 6); row_hi = 16 * (h_tile_base(f0 + 1) + 6); }
        }
        const char* w = down ? a.wd : a.wo;
#pragma unroll
        for (int it = 0; it < 10; ++it) {
            const int i = (it * 4 + wid) * 64 + lane, row = i / 80, c = i % 80;
            const int wrow = row < 16 ? row_lo + row : row_hi + row - 16;
            prep_dma(w + (size_t)wrow * 1280 + c * 16, img + (it * 4 + wid) * 1024);
        }
        wait_vm<0>(); __syncthreads(); PSTAMP(pt1);
        const unsigned rs = 1280;
#pragma unroll
        for (int e = 0; e < 2; ++e) {
            const int c8 = wid * 2 + e;
            const unsigned rowaddr = imgaddr + (8 * g + q) * rs + c8 * 160;
            const int pa = (c8 & 1) ? 16 : 0, hf = (c8 & 1) ? 0 : 64;
            unsigned ad[5];
#pragma unroll
            for (int nt = 0; nt < 5; ++nt) ad[nt] = rowaddr + 2 * (nt < 4 ? pa + 32 * (nt >> 1) + 8 * p + 4 * (nt & 1) : hf + 4 * p);
            bf16x8 fr[5];
            tr_frags<5>(fr, ad, rs);
            char* dst = a.ws + (down ? WS_WD + (size_t)((c8 * 4 + kq) * 14 + s) * 5 * FRAG : WS_WO + (size_t)((c8 * 4 + kq) * 5 + s) * 5 * FRAG);
#pragma unroll
            for (int nt = 0; nt < 5; ++nt) prep_store(dst + nt * FRAG, fr[nt], lane);
            if (down && kq >= 2 && s == 12) {
#pragma unroll
                for (int nt = 0; nt < 5; ++nt) prep_store(dst + (5 + nt) * FRAG, zero, lane);
            }
        }
        PSTAMP(pt2); POUT();
    }
}

enum { K_A = 0, K_W = 1, K_X = 2 };
struct Op { int pos, kind, a, b; };
template <int N> struct Sched {
    Op op[N] = {}; int n = 0;
    constexpr void add(int pos, int kind, int a, int b) { op[n] = Op{pos, kind, a, b}; ++n; }
    constexpr void sort() { for (int i = 1; i < n; ++i) { const Op t = op[i]; int j = i - 1; while (j >= 0 && op[j].pos > t.pos) { op[j + 1] = op[j]; --j; } op[j + 1] = t; } }
    constexpr int before(int pos) const { int c = 0; for (int i = 0; i < n; ++i) c += op[i].pos < pos; return c; }
    constexpr int find(int kind, int a, int b) const { for (int i = 0; i < n; ++i) if (op[i].kind == kind && op[i].a == a && op[i].b == b) return i; return -1; }
    constexpr int wait(int pos, int kind, int a, int b) const { const int i = find(kind, a, b); if (i < 0) return 63; const int w = before(pos) - i - 1; return w < 0 ? -1 : (w > 63 ? 63 : w); }
};
constexpr int cmin(int a, int b) { return a < b ? a : b; }

constexpr int NSLOT = 6, SLOT = 20 * FRAG;
constexpr int V_P1 = 0, V_P3 = 12, V_END = 30;
constexpr int slot_of(int v) { return (v % NSLOT) * SLOT; }

enum { X_STAT = 0, X_KVST = 1, X_HST = 2, X_FLAG = 3, X_CS = 4, X_RES = 5, X_WP = 6 };

template <bool FIRST> constexpr Sched<160> make_p1() {
    Sched<160> s;
    s.add(-1, K_X, X_STAT, 0); s.add(-1, K_X, X_STAT, 1);
    if (FIRST) {
        for (int c = 0; c < 6; ++c) {
            for (int p = 0; p < 5; ++p) s.add(-1, K_W, c, p);
            if (c < 4) for (int k = 2 * c; k < 2 * c + 2; ++k) { s.add(-1, K_A, k, 0); s.add(-1, K_A, k, 1); }
        }
        for (int i = 0; i < 40; ++i) s.add(59 + i, K_X, X_CS, i);
        for (int i = 0; i < 3; ++i) s.add(90 + 2 * i, K_X, X_RES, i);
    } else {
        for (int k = 0; k < 8; ++k) { s.add(-1, K_A, k, 0); s.add(-1, K_A, k, 1); }
    }
    for (int k = 8; k < 20; ++k) { s.add(5 * (k - 8), K_A, k, 0); s.add(5 * (k - 8) + 2, K_A, k, 1); }
    for (int c = 0; c < 3; ++c) for (int p = 0; p < 5; ++p) s.add(61 + (c * 5 + p) * 5 / 2, K_W, c < 2 ? 6 + c : V_P3 + 2, p);
    s.sort(); return s;
}
constexpr Sched<128> make_p1b() {
    Sched<128> s;
    s.add(-1, K_X, X_KVST, 0);
    for (int j = 0; j < 25; ++j) s.add(6 + 2 * j, K_X, X_WP, j);
    s.sort(); return s;
}
constexpr Sched<128> make_p2() {
    Sched<128> s;
    for (int k = 0; k < 3; ++k) for (int m = 0; m < 4; ++m) s.add(-1, K_A, k, m);
    for (int k = 3; k < 5; ++k) for (int m = 0; m < 4; ++m) s.add(((k - 3) * 4 + m) * 4 + 1, K_A, k, m);
    const int ch[2] = {V_P3 + 3, V_P3};
    for (int c = 0; c < 2; ++c) for (int p = 0; p < 5; ++p) s.add(40 + (c * 5 + p) * 6, K_W, ch[c], p);
    s.sort(); return s;
}
constexpr int W4_A = 12, W4_B = 57;
constexpr Sched<192> make_p3() {
    Sched<192> s;
    s.add(-1, K_X, X_STAT, 0); s.add(-1, K_X, X_STAT, 1);
    for (int k = 0; k < 6; ++k) { s.add(-1, K_A, k, 0); s.add(-1, K_A, k, 1); }
    for (int k = 6; k < 20; ++k) { s.add(8 * (k - 6) + 1, K_A, k, 0); s.add(8 * (k - 6) + 5, K_A, k, 1); }
    for (int c = 2; c < 10; ++c) for (int p = 0; p < 5; ++p) { int pos = 20 * c - 6 + 1 + p * 4; if (c == 8 && pos < 160) pos = 160; s.add(pos, K_W, V_P3 + c + 4, p); }
    for (int j = 0; j < W4_A; ++j) s.add(113 + 4 * j, K_X, X_WP, j);
    for (int i = 0; i < 4; ++i) s.add(159, K_X, X_HST, i);
    for (int j = W4_A; j < W4_B; ++j) s.add(162 + ((j - W4_A) * 114) / (W4_B - W4_A), K_X, X_WP, j);
    s.add(250, K_X, X_FLAG, 0);
    s.sort(); return s;
}
constexpr int P4_LA = 3;
constexpr Sched<160> make_p4() {
    Sched<160> s;
    for (int i = 0; i < 3; ++i) s.add(-2, K_X, X_HST, i);
    for (int k = 0; k < P4_LA; ++k) for (int m = 0; m < 4; ++m) s.add(-1, K_A, k, m);
    for (int k = P4_LA; k < 14; ++k) for (int m = 0; m < 4; ++m) s.add(20 * (k - P4_LA) + 1 + 4 * m, K_A, k, m);
    for (int j = W4_B; j < 70; ++j) { const int t = j - W4_B; s.add(t < 11 ? 20 * t + 17 : 20 * 11 + 1 + 4 * (t - 11), K_X, X_WP, j); }
    s.add(20 * (8 - P4_LA) - 21, K_X, X_FLAG, 0);
    s.sort(); return s;
}
template <bool FIRST> constexpr Sched<160> SP1 = make_p1<FIRST>();
constexpr auto SP1B = make_p1b();
constexpr auto SP3 = make_p3();
constexpr auto SP2 = make_p2();
constexpr auto SP4 = make_p4();

constexpr int L_RING = 0, L_SCR = NSLOT * SLOT, L_IMG = L_SCR + 8 * FRAG, L_XCH = 4 * SLOT, L_TAB = L_SCR + 32 * FRAG, L_CSQ = L_TAB + 1024, L_CSG = L_CSQ + 512, L_STAMP = L_CSG + 1024,
              LDS_BYTES = L_STAMP + 512;
struct MainArgs { char* ws; char* out; };

DI void row_stats(const u32x4& p, float& rstd, float& mrs) {
    float s = __uint_as_float(p[0]) + __uint_as_float(p[2]), s2 = __uint_as_float(p[1]) + __uint_as_float(p[3]);
    s = xg_sum(s); s2 = xg_sum(s2);
    const float mean = s * (1.f / DM), var = fmaxf(s2 * (1.f / DM) - mean * mean, 0.f);
    rstd = __builtin_amdgcn_rsqf(var + 1e-5f);
    mrs = mean * rstd;
}
DI void spin_ge(const char* ws, int line, unsigned target) {
    gu32* p = (gu32*)(ws + WS_CTL + (size_t)line * 256);
    gu32* tmo = (gu32*)(ws + WS_CTL + C_TMO * 256);
    unsigned spins = 0;
    while ((unsigned)__builtin_amdgcn_readfirstlane(__hip_atomic_load(p, __ATOMIC_RELAXED, __HIP_MEMORY_SCOPE_AGENT)) < target) {
        __builtin_amdgcn_s_sleep(1);
        ++spins;
        if ((spins & 1023u) == 0u && __builtin_amdgcn_readfirstlane(__hip_atomic_load(tmo, __ATOMIC_RELAXED, __HIP_MEMORY_SCOPE_AGENT))) break;
        if (spins > (1u << 17)) { __hip_atomic_store(tmo, 1u, __ATOMIC_RELAXED, __HIP_MEMORY_SCOPE_AGENT); break; }
    }
    asm volatile("" ::: "memory");
}
DI void signal(char* ws, int line) { __hip_atomic_fetch_add((gu32*)(ws + WS_CTL + (size_t)line * 256), 1u, __ATOMIC_RELAXED, __HIP_MEMORY_SCOPE_AGENT); }
template <bool FIRST> DI void mma_av(f32x4& c, const bf16x8& w, const bf16x8& x) {
    if constexpr (FIRST) asm volatile("v_mfma_f32_16x16x32_bf16 %0, %1, %2, 0" : "=v"(c) : "a"(w), "v"(x));
    else asm volatile("v_mfma_f32_16x16x32_bf16 %0, %1, %2, %0" : "+v"(c) : "a"(w), "v"(x));
}
template <int OFF> DI void ld_x(bf16x8& d, const char* sb, unsigned voff) { asm volatile("global_load_dwordx4 %0, %1, %2 offset:%3 sc1" : "=v"(d) : "v"(voff), "s"(sb), "i"(OFF) : "memory"); }
template <int OFF> DI void ds_wr(unsigned addr, const f32x4& v) { asm volatile("ds_write_b128 %0, %1 offset:%2" ::"v"(addr), "v"(v), "i"(OFF) : "memory"); }
template <int OFF> DI void ld_w(bf16x8& d, const char* sb, unsigned voff) { asm volatile("global_load_dwordx4 %0, %1, %2 offset:%3" : "=a"(d) : "v"(voff), "s"(sb), "i"(OFF) : "memory"); }

__global__ __launch_bounds__(256, 1) void main_kernel(MainArgs a) {
    extern __shared__ __attribute__((aligned(16))) char shm[];
    const unsigned lds0 = (unsigned)(size_t)shm;
    LAS char* lds = (LAS char*)shm;
    char* const ws = a.ws;
    const int tid = threadIdx.x, wid = __builtin_amdgcn_readfirstlane(tid >> 6), lane = tid & 63, g = lane >> 4, r = lane & 15;
    const unsigned voff = lane * 16;
    int rp, cg; map16x16(blockIdx.x, rp, cg);
    int rt, c8; map32x8(blockIdx.x, rt, c8);
    const int mt_a = rp * 8 + wid * 2;
    const int mtf = rt * 4 + wid, odd = c8 & 1;
    const int tA = odd ? (5 * c8 + 1) / 2 : 5 * c8 / 2, tH = odd ? (5 * c8 - 1) / 2 : 5 * c8 / 2 + 2;
    const unsigned hoff = tH * FRAG + (odd * 2 + (g >> 1)) * 256 + r * 16 + (g & 1) * 8;
    STAMP(0);

    const char* wP1 = ws + WS_WQKV + (size_t)(cg * 160 + wid * 5) * FRAG;
    const char* wP3 = ws + WS_WGU + (size_t)(cg * 280 + wid * 5) * FRAG;
    const char* wP2 = ws + WS_WO + (size_t)((c8 * 4 + wid) * 25) * FRAG;
    const char* wP4 = ws + WS_WD + (size_t)((c8 * 4 + wid) * 70) * FRAG;
    unsigned wdst = lds0 + L_RING + wid * 5 * FRAG;
    auto fresh_ptrs = [&]() { pins(wP1); pins(wP2); pins(wP3); pins(wP4); pins(wdst); };
    auto w_op = [&](auto V, auto P) {
        constexpr int v0 = V, v = v0 % V_END, p = P;
        const char* src = v < V_P3 ? wP1 + (size_t)(v - V_P1) * SLOT : wP3 + (size_t)(v - V_P3) * SLOT;
        dma_piece<p>(src, wdst + slot_of(v), voff);
    };
    auto w_chunk = [&](auto V) { sfor<0, 5>([&](auto P) { w_op(V, P); }); };
    const unsigned rdb = lds0 + L_RING + voff, rdb_hi = rdb + 65536;

    u32x4 resA, resB; u32x2 resH;
    float csk[5]; f32x4 csq[3];
    bf16x8 W2[25], W4[70];

    for (int l = 0; l < 2; ++l) {
        {
            const char* xsrc = ws + WS_X + (size_t)(2 * l) * XT_BYTES + (size_t)mt_a * KSD * FRAG;
            const char* stsrc = ws + WS_ST + (size_t)(2 * l) * ST_BYTES + (size_t)mt_a * 16 * 64;
            char* kvp = ws + WS_KVP + (size_t)((l * 16 + cg) * 16) * KVP_SLOT;
            if (l > 0) { if (wid == 0) spin_ge(ws, C_X2 + rp, 16u); wait_vm<0>(); bar(); }
            STAMP(l * 24 + 1);
            fresh_ptrs(); pins(xsrc);
            bf16x8 A[2][20];
            f32x4 acc1[2][5], acc2[2][3];
            float rstd[2], mrs[2];
            f32x4 st[2][2];
            auto pass1 = [&](auto FST) {
                constexpr bool first = FST;
                u32x4 stp[2]; unsigned cq[20], cgp[20];
                const char* csqb = ws + WS_CSQ + (size_t)cg * 20 * 512; const char* csgb = ws + WS_CSG + (size_t)cg * 20 * 896; const char* xr = ws + WS_X + (size_t)mtf * KSD * FRAG;
                pins(csqb); pins(csgb); pins(xr);
                auto emit = [&](auto K) {
                    constexpr Op o = SP1<first>.op[K];
                    if constexpr (o.kind == K_A) ld_a<(o.a % 4) * FRAG>(A[o.b][o.a], xsrc + (size_t)(o.b * KSD + (o.a / 4) * 4) * FRAG, voff);
                    else if constexpr (o.kind == K_W) w_op(IC<o.a>{}, IC<o.b>{});
                    else if constexpr (o.a == X_STAT) ld_v<o.b * FRAG>(stp[o.b], stsrc, r * 64 + g * 16);
                    else if constexpr (o.a == X_CS) {
                        if constexpr (o.b < 20) ld_v1<(o.b % 8) * 512>(cq[o.b], csqb + (o.b / 8) * 4096, (tid & 127) * 4);
                        else ld_v1<((o.b - 20) % 4) * 896>(cgp[o.b - 20], csgb + ((o.b - 20) / 4) * 3584, (tid < 224 ? tid : 0) * 4);
                    } else if constexpr (o.a == X_RES) {
                        if constexpr (o.b == 0) ld_v<0>(resA, xr + tA * FRAG, voff); else if constexpr (o.b == 1) ld_v<FRAG>(resB, xr + tA * FRAG, voff); else ld_v2(resH, xr, hoff);
                    }
                };
                sfor<0, SP1<first>.before(0)>(emit);
                wait_vm<SP1<first>.wait(0, K_X, X_STAT, 1)>();
                pin(stp[0]); pin(stp[1]);
                row_stats(stp[0], rstd[0], mrs[0]); row_stats(stp[1], rstd[1], mrs[1]);
                if (g == 0) {
                    *(LAS f32x2*)(lds + L_TAB + (wid * 32 + r) * 8) = (f32x2){rstd[0], mrs[0]};
                    *(LAS f32x2*)(lds + L_TAB + (wid * 32 + 16 + r) * 8) = (f32x2){rstd[1], mrs[1]};
                }
                sfor<0, 2>([&](auto M) { st[M][0] = *(LAS f32x4*)(lds + L_TAB + (wid * 32 + M * 16 + 4 * g) * 8); st[M][1] = *(LAS f32x4*)(lds + L_TAB + (wid * 32 + M * 16 + 4 * g) * 8 + 16); });
                pin(st[0][0]); pin(st[0][1]); pin(st[1][0]); pin(st[1][1]);
                constexpr int L = 4, R = 5;
                bf16x8 B[R];
                auto rd = [&](auto I) { constexpr int i = I; if constexpr (i < 100) ds_rd_at<slot_of(V_P1 + i / 20) + (i % 20) * FRAG>(B[i % R], rdb, rdb_hi); };
                if constexpr (first) { wait_vm<SP1<first>.wait(0, K_W, 0, 4)>(); bar(); }
                sfor<0, L>(rd);
                wait_lgkm<L - 1>();
                sfor<0, 100>([&](auto I) {
                    constexpr int i = I, ks = i / 5, n = i % 5;
                    if constexpr (n == 0) wait_vm<SP1<first>.wait(i, K_A, ks, 1)>();
                    if constexpr (first && i % 20 == 20 - L && i < 80) { wait_vm<SP1<first>.wait(i, K_W, i / 20 + 1, 4)>(); bar(); }
                    if constexpr (i == 60) bar();
                    mma_t<ks == 0>(acc1[0][n], B[i % R], A[0][ks]);
                    rd(IC<i + L>{});
                    mma_t<ks == 0>(acc1[1][n], B[i % R], A[1][ks]);
                    sfor<SP1<first>.before(i), SP1<first>.before(i + 1)>(emit);
                    if constexpr (i + 1 < 100) wait_lgkm<cmin(L - 1, 98 - i)>();
                });
                if constexpr (first) {
                    wait_vm<cmin(SP1<first>.n - SP1<first>.find(K_X, X_RES, 2) - 1, SP1<first>.n - SP1<first>.find(K_X, X_CS, 39) - 1)>();
                    float sq = 0.f, sg = 0.f;
                    sfor<0, 20>([&](auto J) { constexpr int j = J; pin(cq[j]); pin(cgp[j]); sq += __uint_as_float(cq[j]); sg += __uint_as_float(cgp[j]); });
                    pin(resA); pin(resB); pin(resH);
                    if (tid < 128) *(LAS float*)(lds + L_CSQ + tid * 4) = sq;
                    if (tid < 224) *(LAS float*)(lds + L_CSG + tid * 4) = sg;
                    wait_lgkm<0>(); bar();
                    sfor<0, 5>([&](auto N) { csk[N] = *(LAS float*)(lds + L_CSQ + (N * 16 + r) * 4); });
                    sfor<0, 3>([&](auto N) { csq[N] = *(LAS f32x4*)(lds + L_CSQ + ((5 + N) * 16 + 4 * g) * 4); });
                }
            };
            if (l == 0) pass1(std::true_type{}); else pass1(std::false_type{});
            mma_settle();
            sfor<0, 5>([&](auto N) { mma_fence(acc1[0][N]); mma_fence(acc1[1][N]); });
            STAMP(l * 24 + 2);
            {
                u32x4 op[5];
                sfor<0, 5>([&](auto N) {
                    constexpr int n = N;
                    float v[2][4];
                    sfor<0, 2>([&](auto M) {
                        sfor<0, 4>([&](auto J) { constexpr int m = M, j = J;
                            float t = acc1[m][n][j] * st[m][j >> 1][(j & 1) * 2] - st[m][j >> 1][(j & 1) * 2 + 1] * csk[n];
                            if constexpr (n < 2) t = phi(t); else if constexpr (n == 2) t = r < 8 ? phi(t) : t;
                            v[m][j] = t; });
                    });
                    op[n] = (u32x4){pk_bf16(v[0][0], v[0][1]), pk_bf16(v[0][2], v[0][3]), pk_bf16(v[1][0], v[1][1]), pk_bf16(v[1][2], v[1][3])};
                });
                u32x4 v2 = op[2];
                if (r < 8) v2 = (u32x4){0x3f803f80u, 0x3f803f80u, 0x3f803f80u, 0x3f803f80u};
                const f32x4 z4 = {0.f, 0.f, 0.f, 0.f};
                f32x4 kv[8];
                kv[0] = mfma_c(op[0], op[3], z4); kv[1] = mfma_c(op[0], op[4], z4); kv[2] = mfma_c(op[0], v2, z4);
                kv[3] = mfma_c(op[1], op[3], z4); kv[4] = mfma_c(op[1], op[4], z4); kv[5] = mfma_c(op[1], v2, z4);
                kv[6] = mfma_c(op[2], op[4], z4); kv[7] = mfma_c(op[2], v2, z4);
                sfor<0, 8>([&](auto T) { *(LAS f32x4*)(lds + L_SCR + (wid * 8 + T) * FRAG + voff) = kv[T]; });
            }
            wait_lgkm<0>(); bar();
            int s_dg, s_e; bool s_z;
            { const int t = tid < 210 ? tid : 209; s_z = t >= 200; const int hd = t / 100, v = t % 100; s_dg = s_z ? t - 200 : hd * 5 + v / 20; s_e = s_z ? 0 : hd * 20 + v % 20; }
            const unsigned kvo = (tid < 210 ? tid : 209) * 16;
            {
                const int dt = s_dg >> 2, gg = s_dg & 3;
                const int et = s_z ? 2 : s_e >> 4, cc = s_z ? 0 : (s_e < 32 ? s_e & 15 : s_e - 24);
                const int T = dt == 2 ? 5 + et : dt * 3 + et;
                const unsigned o = L_SCR + T * FRAG + (gg * 16 + cc) * 16;
                const f32x4 sum = (*(LAS f32x4*)(lds + o) + *(LAS f32x4*)(lds + o + 8 * FRAG)) + (*(LAS f32x4*)(lds + o + 16 * FRAG) + *(LAS f32x4*)(lds + o + 24 * FRAG));
                st_v(kvp + rp * KVP_SLOT, kvo, __builtin_bit_cast(u32x4, sum));
            }
            STAMP(l * 24 + 3);
            fresh_ptrs();
            {
                constexpr int R2 = 6, L2 = 4;
                bf16x8 B2[R2];
                auto rd = [&](auto I) { constexpr int i = I; if constexpr (i < 60) ds_rd_at<slot_of(V_P1 + 5 + i / 20) + (i % 20) * FRAG>(B2[i % R2], rdb, rdb_hi); };
                auto emit = [&](auto K) { constexpr Op o = SP1B.op[K]; if constexpr (o.a == X_WP) ld_w<(o.b % 4) * FRAG>(W2[o.b], wP2 + (size_t)(o.b / 4) * 4 * FRAG, voff); };
                sfor<0, L2>(rd);
                wait_lgkm<L2 - 1>();
                sfor<0, 60>([&](auto I) {
                    constexpr int i = I, ks = i / 3, n = i % 3;
                    if constexpr (i == 20 - L2) {
                        wait_vm<SP1B.wait(i, K_X, X_KVST, 0)>(); bar();
                        if (tid == 0) signal(ws, C_KV + l * 16 + cg);
                        *(LAS u32x4*)(lds + L_IMG + tid * 16) = (u32x4){0u, 0u, 0u, 0u};
                        if (tid < 128) *(LAS u32x4*)(lds + L_IMG + 4096 + tid * 16) = (u32x4){0u, 0u, 0u, 0u};
                    }
                    mma<ks == 0>(acc2[0][n], B2[i % R2], A[0][ks]);
                    rd(IC<i + L2>{});
                    mma<ks == 0>(acc2[1][n], B2[i % R2], A[1][ks]);
                    sfor<SP1B.before(i), SP1B.before(i + 1)>(emit);
                    if constexpr (i + 1 < 60) wait_lgkm<cmin(L2 - 1, 58 - i)>();
                });
            }
            mma_settle();
            sfor<0, 3>([&](auto N) { mma_fence(acc2[0][N]); mma_fence(acc2[1][N]); });
            STAMP(l * 24 + 4);
            if (wid == 0) spin_ge(ws, C_KV + l * 16 + cg, 16u);
            wait_lgkm<0>(); bar();
            STAMP(l * 24 + 5);
            u32x4 pv[16];
            sfor<0, 16>([&](auto I) { ld_v<0>(pv[I], kvp + I * KVP_SLOT, kvo); });
            u32x4 qa[2], qb[2];
            sfor<0, 2>([&](auto M) {
                constexpr int m = M;
                unsigned pk[3][2];
                sfor<0, 3>([&](auto N) {
                    constexpr int n = N;
                    float v[4];
                    sfor<0, 4>([&](auto J) { constexpr int j = J; v[j] = phi(acc2[m][n][j] * rstd[m] - mrs[m] * csq[n][j]); });
                    pk[n][0] = pk_bf16(v[0], v[1]); pk[n][1] = pk_bf16(v[2], v[3]);
                });
                qa[m] = (u32x4){pk[0][0], pk[0][1], pk[2][0], pk[2][1]};
                qb[m] = (u32x4){pk[1][0], pk[1][1], pk[2][0], pk[2][1]};
            });
            pin(qa[0]); pin(qa[1]); pin(qb[0]); pin(qb[1]);
            {
                wait_vm<0>();
                sfor<0, 16>([&](auto I) { pin(pv[I]); });
                f32x4 s0 = __builtin_bit_cast(f32x4, pv[0]) + __builtin_bit_cast(f32x4, pv[1]), s1 = __builtin_bit_cast(f32x4, pv[2]) + __builtin_bit_cast(f32x4, pv[3]);
#pragma unroll
                for (int i = 4; i < 16; i += 2) { s0 += __builtin_bit_cast(f32x4, pv[i]); s1 += __builtin_bit_cast(f32x4, pv[i + 1]); }
                const f32x4 s = s0 + s1;
                const u32x2 w = {pk_bf16(s[0], s[1]), pk_bf16(s[2], s[3])};
                const int hd = s_dg >= 5, dl = s_dg - hd * 5;
                const int gg = dl < 4 ? dl : hd, hb = dl < 4 ? 0 : 8;
                if (tid < 200) {
                    const int e = s_e, T = e >= 32 ? 2 : (e >> 2) & 1, c = e >= 32 ? e - 32 : (e >> 3) * 4 + (e & 3);
                    const int img = T == 2 ? 4 : T * 2 + hd;
                    *(LAS u32x2*)(lds + L_IMG + img * FRAG + (gg * 16 + c) * 16 + hb) = w;
                } else if (tid < 210) {
                    *(LAS u32x2*)(lds + L_IMG + 5 * FRAG + hd * 64 + gg * 16 + hb) = w;
                }
            }
            wait_lgkm<0>(); bar();
            STAMP(l * 24 + 6);
            {
                u32x4 im[5];
                sfor<0, 5>([&](auto T) { im[T] = *(LAS u32x4*)(lds + L_IMG + T * FRAG + voff); });
                const u32x4 da = *(LAS u32x4*)(lds + L_IMG + 5 * FRAG + g * 16), db = *(LAS u32x4*)(lds + L_IMG + 5 * FRAG + 64 + g * 16);
                const f32x4 z4 = {0.f, 0.f, 0.f, 0.f};
                const char* obase = ws + WS_O + (size_t)l * XT_BYTES + (size_t)mt_a * KSD * FRAG;
                u32x2 t2p[2];
                sfor<0, 2>([&](auto M) {
                    constexpr int m = M;
                    f32x4 t0 = mfma_c(im[0], qa[m], z4); t0 = mfma_c(im[1], qb[m], t0);
                    f32x4 t1 = mfma_c(im[2], qa[m], z4); t1 = mfma_c(im[3], qb[m], t1);
                    const f32x4 t2 = mfma_c(im[4], qb[m], z4);
                    const f32x4 dA = mfma_c(da, qa[m], z4), dB = mfma_c(db, qb[m], z4);
                    const float ia = 1.f / (dA[0] + 1e-6f), ib = 1.f / (dB[0] + 1e-6f);
                    const float i0 = g < 3 ? ia : ib, i1 = g < 2 ? ia : ib;
                    const u32x4 w = {pk_bf16(t0[0] * i0, t0[1] * i0), pk_bf16(t0[2] * i0, t0[3] * i0), pk_bf16(t1[0] * i1, t1[1] * i1), pk_bf16(t1[2] * i1, t1[3] * i1)};
                    const int G = 5 * cg + g;
                    st_v(obase, (m * KSD + (G >> 2)) * FRAG + (G & 3) * 256 + r * 16, w);
                    t2p[m] = (u32x2){pk_bf16(t2[0] * ib, t2[1] * ib), pk_bf16(t2[2] * ib, t2[3] * ib)};
                });
                const auto s0 = __builtin_amdgcn_permlane16_swap(t2p[0][0], t2p[1][0], false, false);
                const auto s1 = __builtin_amdgcn_permlane16_swap(t2p[0][1], t2p[1][1], false, false);
                const u32x4 w = {s0[0], s1[0], s0[1], s1[1]};
                const int G = 5 * cg + 4;
                if (g < 2) st_v(obase, (g * KSD + (G >> 2)) * FRAG + (G & 3) * 256 + r * 16, w);
            }
            wait_vm<0>(); bar();
            if (tid == 0) signal(ws, C_O + l * 16 + rp);
            STAMP(l * 24 + 7);
        }
        auto proj = [&](auto PH) {
            constexpr int ph = PH;
            constexpr int NS = ph == 2 ? 5 : 14, KS = ph == 2 ? KSD : KSF;
            const char* abase = ws + (ph == 2 ? WS_O + (size_t)l * XT_BYTES : WS_H + (size_t)l * HT_BYTES) + (size_t)(rt * 4 * KS + wid * NS) * FRAG;
            const char* asrc[4]; const char* alast[4];
            sfor<0, 4>([&](auto M) { asrc[M] = abase + (size_t)(((wid + M) & 3) * KS) * FRAG; pins(asrc[M]);
                alast[M] = asrc[M] + (size_t)((ph == 4 && wid >= 2) ? 12 : NS - 1) * FRAG; });
            fresh_ptrs();
            constexpr int AW = ph == 2 ? 5 : P4_LA + 1;
            bf16x8 A[AW][4];
            f32x4 acc[4][5];
            unsigned flagv;
            auto emit = [&](auto K) {
                constexpr Op o = ph == 2 ? SP2.op[K] : SP4.op[K];
                if constexpr (o.kind == K_A) {
                    if constexpr (o.a == NS - 1) ld_x<0>(A[o.a % AW][o.b], alast[o.b], voff);
                    else ld_x<(o.a % 4) * FRAG>(A[o.a % AW][o.b], asrc[o.b] + (size_t)(o.a / 4) * 4 * FRAG, voff);
                }
                else if constexpr (o.kind == K_W) w_op(IC<o.a>{}, IC<o.b>{});
                else if constexpr (o.a == X_WP) ld_w<(o.b % 4) * FRAG>(W4[o.b], wP4 + (size_t)(o.b / 4) * 4 * FRAG, voff);
                else if constexpr (o.a == X_FLAG) ld_v1(flagv, ws + WS_CTL + (size_t)(C_HB + l * 16 + (rt >> 1)) * 256, 0u);
            };
            constexpr int first = ph == 2 ? 0 : 3;
            constexpr int npro = ph == 2 ? SP2.before(0) : SP4.before(0);
            sfor<first, npro>(emit);
            unsigned xw[3];
            sfor<0, 3>([&](auto M) { xw[M] = lds0 + L_XCH + ((((wid + M + 1) & 3) * 3 + M) * 5) * FRAG + voff; });
            sfor<0, NS * 20>([&](auto I) {
                constexpr int i = I, s = i / 20;
                constexpr bool last = s == NS - 1;
                constexpr int n = last ? i % 5 : (i % 20) / 4, m = last ? (((i % 20) / 5 + 1) & 3) : i % 4;
                if constexpr (i % 20 == 0) {
                    constexpr int wa = ph == 2 ? SP2.wait(i, K_A, s, 3) : SP4.wait(i, K_A, s, 3);
                    constexpr int ww = ph == 2 ? 63 : SP4.wait(i, K_X, X_WP, s * 5 + 4);
                    wait_vm<cmin(wa, ww)>();
                    if constexpr (ph == 4 && i == 0) { bar(); if (tid == 0) signal(ws, C_HB + l * 16 + rp); }
                }
                if constexpr (ph == 4 && i == 20 * (8 - P4_LA)) {
                    wait_vm<SP4.wait(i, K_X, X_FLAG, 0)>();
                    pin(flagv);
                    if (__builtin_amdgcn_readfirstlane(flagv) < 16) spin_ge(ws, C_HB + l * 16 + (rt >> 1), 16u);
                }
                if constexpr (ph == 2) mma_av<s == 0>(acc[m][n], W2[s * 5 + n], A[s % AW][m]);
                else mma_av<s == 0>(acc[m][n], W4[s * 5 + n], A[s % AW][m]);
                if constexpr (ph == 2) sfor<SP2.before(i), SP2.before(i + 1)>(emit); else sfor<SP4.before(i), SP4.before(i + 1)>(emit);
                if constexpr (last && i % 20 >= 10) { constexpr int q = i % 20 - 10, t = q / 5 + 1; ds_wr<(q % 5) * FRAG>(xw[t - 1], acc[t][q % 5]); }
            });
            mma_settle();
            sfor<0, 4>([&](auto M) { sfor<0, 5>([&](auto N) { mma_fence(acc[M][N]); }); });
            STAMP(l * 24 + (ph == 2 ? 9 : 17));
            sfor<0, 5>([&](auto N) { ds_wr<N * FRAG>(xw[2], acc[3][N]); });
            wait_lgkm<0>(); bar();
            f32x4 fin[5];
            sfor<0, 5>([&](auto N) {
                const unsigned o = L_XCH + (wid * 15 + N) * FRAG + voff;
                fin[N] = (acc[0][N] + *(LAS f32x4*)(lds + o)) + (*(LAS f32x4*)(lds + o + 5 * FRAG) + *(LAS f32x4*)(lds + o + 10 * FRAG));
            });
            auto fin_pair = [&](const f32x4& lo, const f32x4& hi, const u32x4& x) {
                u32x4 w;
                w[0] = pk_bf16(lo[0] + bf_lo(x[0]), lo[1] + bf_hi(x[0])); w[1] = pk_bf16(lo[2] + bf_lo(x[1]), lo[3] + bf_hi(x[1]));
                w[2] = pk_bf16(hi[0] + bf_lo(x[2]), hi[1] + bf_hi(x[2])); w[3] = pk_bf16(hi[2] + bf_lo(x[3]), hi[3] + bf_hi(x[3]));
                return w;
            };
            resA = fin_pair(fin[0], fin[1], resA); resB = fin_pair(fin[2], fin[3], resB);
            resH = (u32x2){pk_bf16(fin[4][0] + bf_lo(resH[0]), fin[4][1] + bf_hi(resH[0])), pk_bf16(fin[4][2] + bf_lo(resH[1]), fin[4][3] + bf_hi(resH[1]))};
            if (ph == 4 && l == 1) {
                char* row = a.out + (size_t)(mtf * 16 + r) * 1280;
                *(GAS u32x4*)(row + tA * 64 + g * 16) = resA;
                *(GAS u32x4*)(row + tA * 64 + 64 + g * 16) = resB;
                *(GAS u32x2*)(row + tH * 64 + odd * 32 + g * 8) = resH;
            } else {
                const int xo = 2 * l + (ph == 2 ? 1 : 2);
                const char* xb = ws + WS_X + (size_t)xo * XT_BYTES + (size_t)mtf * KSD * FRAG;
                st_v(xb + tA * FRAG, voff, resA); st_v(xb + (tA + 1) * FRAG, voff, resB); st_v2(xb, hoff, resH);
                float s1 = 0.f, s2 = 0.f;
                auto accum = [&](unsigned u) { const float lo = bf_lo(u), hi = bf_hi(u); s1 += lo + hi; s2 += lo * lo + hi * hi; };
                sfor<0, 4>([&](auto J) { constexpr int j = J; accum(resA[j]); accum(resB[j]); });
                accum(resH[0]); accum(resH[1]);
                s1 = xg_sum(s1); s2 = xg_sum(s2);
                if (g == 0) st_v2(ws + WS_ST + (size_t)xo * ST_BYTES + (size_t)mtf * 16 * 64, r * 64 + c8 * 8, (u32x2){__float_as_uint(s1), __float_as_uint(s2)});
                wait_vm<0>(); bar();
                if (tid == 0) signal(ws, (ph == 2 ? C_X1 + l * 16 : C_X2) + (rt >> 1));
                fresh_ptrs();
                if constexpr (ph == 2) { w_chunk(IC<V_P3 + 1>{}); w_chunk(IC<V_P3 + 4>{}); w_chunk(IC<V_P3 + 5>{}); }
                else sfor<0, 6>([&](auto V) { w_chunk(IC<V_END + V>{}); });
            }
        };
        if (wid == 0) spin_ge(ws, C_O + l * 16 + (rt >> 1), 16u);
        bar();
        STAMP(l * 24 + 8);
        proj(IC<2>{});
        STAMP(l * 24 + 10);
        {
            const char* xsrc = ws + WS_X + (size_t)(2 * l + 1) * XT_BYTES + (size_t)mt_a * KSD * FRAG;
            const char* stsrc = ws + WS_ST + (size_t)(2 * l + 1) * ST_BYTES + (size_t)mt_a * 16 * 64;
            const char* hb = ws + WS_H + (size_t)l * HT_BYTES + (size_t)mt_a * KSF * FRAG;
            if (wid == 0) spin_ge(ws, C_X1 + l * 16 + rp, 16u);
            wait_vm<0>(); bar();
            STAMP(l * 24 + 11);
            fresh_ptrs(); pins(xsrc);
            bf16x8 A[2][20];
            f32x4 accA[2][8], accB[2][6];
            u32x4 stp[2]; unsigned flagv;
            auto emit = [&](auto K) {
                constexpr Op o = SP3.op[K];
                if constexpr (o.kind == K_A) ld_a<(o.a % 4) * FRAG>(A[o.b][o.a], xsrc + (size_t)(o.b * KSD + (o.a / 4) * 4) * FRAG, voff);
                else if constexpr (o.kind == K_W) w_op(IC<o.a>{}, IC<o.b>{});
                else if constexpr (o.a == X_STAT) ld_v<o.b * FRAG>(stp[o.b], stsrc, r * 64 + g * 16);
                else if constexpr (o.a == X_WP) ld_w<(o.b % 4) * FRAG>(W4[o.b], wP4 + (size_t)(o.b / 4) * 4 * FRAG, voff);
                else if constexpr (o.a == X_FLAG) ld_v1(flagv, ws + WS_CTL + (size_t)(C_HA + l * 16 + (rt >> 1)) * 256, 0u);
            };
            sfor<0, SP3.before(0)>(emit);
            float rstd[2], mrs[2];
            wait_vm<SP3.wait(0, K_X, X_STAT, 1)>();
            pin(stp[0]); pin(stp[1]);
            row_stats(stp[0], rstd[0], mrs[0]); row_stats(stp[1], rstd[1], mrs[1]);
            pin(rstd[0]); pin(rstd[1]); pin(mrs[0]); pin(mrs[1]);
            constexpr int L = 6, R = 7;
            bf16x8 B[R];
            f32x4 cs[8];
            auto rd = [&](auto I, auto LIM) { constexpr int i = I; if constexpr (i < LIM) ds_rd_at<slot_of(V_P3 + i / 20) + (i % 20) * FRAG>(B[i % R], rdb, rdb_hi); };
            sfor<0, L>([&](auto I) { rd(I, IC<160>{}); });
            wait_lgkm<L - 1>();
            sfor<0, 160>([&](auto I) {
                constexpr int i = I, ks = i / 8, n = i % 8;
                if constexpr (n == 0) wait_vm<SP3.wait(i, K_A, ks, 1)>();
                if constexpr (i % 20 == 20 - L && i / 20 + 1 >= 2 && i / 20 + 1 < 8) { wait_vm<SP3.wait(i, K_W, V_P3 + i / 20 + 1, 4)>(); bar(); }
                if constexpr (i == 154) sfor<0, 8>([&](auto N) { cs[N] = *(LAS f32x4*)(lds + L_CSG + (N * 16 + 4 * g) * 4); });
                mma<ks == 0>(accA[0][n], B[i % R], A[0][ks]);
                rd(IC<i + L>{}, IC<160>{});
                mma<ks == 0>(accA[1][n], B[i % R], A[1][ks]);
                sfor<SP3.before(i), (i == 159 ? SP3.find(K_X, X_HST, 0) : SP3.before(i + 1))>(emit);
                if constexpr (i + 1 < 160) wait_lgkm<cmin(L - 1, 158 - i)>();
            });
            mma_settle();
            sfor<0, 8>([&](auto N) { mma_fence(accA[0][N]); mma_fence(accA[1][N]); });
            STAMP(l * 24 + 12);
            auto h_tile = [&](const f32x4& ga, const f32x4& ua, const f32x4& cgv, const f32x4& cuv, int m) {
                const f32x4 h = (ga * rstd[m] - cgv * mrs[m]) * (ua * rstd[m] - cuv * mrs[m]);
                return (u32x2){pk_bf16(h[0], h[1]), pk_bf16(h[2], h[3])};
            };
            sfor<0, 2>([&](auto P) {
                sfor<0, 2>([&](auto M) {
                    constexpr int p = P, m = M;
                    const u32x2 lo = h_tile(accA[m][4 * p], accA[m][4 * p + 1], cs[4 * p], cs[4 * p + 1], m), hi = h_tile(accA[m][4 * p + 2], accA[m][4 * p + 3], cs[4 * p + 2], cs[4 * p + 3], m);
                    st_v(hb + (size_t)(m * KSF + h_pos_a(cg, p)) * FRAG, voff, (u32x4){lo[0], lo[1], hi[0], hi[1]});
                });
            });
            STAMP(l * 24 + 13);
            fresh_ptrs();
            wait_vm<SP3.wait(160, K_W, V_P3 + 8, 4)>(); wait_lgkm<0>(); bar();
            sfor<160, 160 + L>([&](auto I) { rd(I, IC<280>{}); });
            wait_lgkm<L - 1>();
            sfor<160, 280>([&](auto I) {
                constexpr int i = I, ks = (i - 160) / 6, n = (i - 160) % 6;
                if constexpr (i % 20 == 20 - L && i / 20 + 1 < 14) {
                    constexpr int wv = SP3.wait(i, K_W, V_P3 + i / 20 + 1, 4);
                    if constexpr (i / 20 + 1 == 10) { wait_vm<cmin(wv, SP3.wait(i, K_X, X_HST, 3))>(); bar(); if (tid == 0) signal(ws, C_HA + l * 16 + rp); }
                    else { wait_vm<wv>(); bar(); }
                }
                if constexpr (i == 274) sfor<0, 6>([&](auto N) { cs[N] = *(LAS f32x4*)(lds + L_CSG + ((8 + N) * 16 + 4 * g) * 4); });
                mma<ks == 0>(accB[0][n], B[i % R], A[0][ks]);
                rd(IC<i + L>{}, IC<280>{});
                mma<ks == 0>(accB[1][n], B[i % R], A[1][ks]);
                sfor<SP3.before(i), SP3.before(i + 1)>(emit);
                if constexpr (i + 1 < 280) wait_lgkm<cmin(L - 1, 278 - i)>();
            });
            mma_settle();
            sfor<0, 6>([&](auto N) { mma_fence(accB[0][N]); mma_fence(accB[1][N]); });
            STAMP(l * 24 + 14);
            wait_vm<SP3.n - SP3.find(K_X, X_FLAG, 0) - 1>();
            pin(flagv);
            u32x2 t6[2];
            sfor<0, 2>([&](auto M) {
                constexpr int m = M;
                const u32x2 lo = h_tile(accB[m][0], accB[m][1], cs[0], cs[1], m), hi = h_tile(accB[m][2], accB[m][3], cs[2], cs[3], m);
                st_v(hb + (size_t)(m * KSF + h_pos_b(cg)) * FRAG, voff, (u32x4){lo[0], lo[1], hi[0], hi[1]});
                t6[m] = h_tile(accB[m][4], accB[m][5], cs[4], cs[5], m);
            });
            {
                const auto s0 = __builtin_amdgcn_permlane16_swap(t6[0][0], t6[1][0], false, false);
                const auto s1 = __builtin_amdgcn_permlane16_swap(t6[0][1], t6[1][1], false, false);
                const u32x4 w = {s0[0], s1[0], s0[1], s1[1]};
                const char* dst = cg < 12 ? hb + (size_t)h_pos_b(16 + (cg >> 1)) * FRAG : ws + WS_DUMMY;
                const unsigned off = cg < 12 ? (g & 1) * KSF * FRAG + (2 * (cg & 1) + (g >> 1)) * 256 + r * 16 : (blockIdx.x * 4 + wid) * FRAG + voff;
                st_v(dst, off, w);
            }
            if (__builtin_amdgcn_readfirstlane(flagv) < 16) spin_ge(ws, C_HA + l * 16 + (rt >> 1), 16u);
            STAMP(l * 24 + 15);
        }
        proj(IC<4>{});
        STAMP(l * 24 + 18);
    }
}

extern "C" void kernel_launch(void* const* d_in, const int* in_sizes, int n_in, void* d_out, int out_size, void* d_ws, size_t ws_size, hipStream_t stream) {
    char* ws = (char*)d_ws;
    (void)hipFuncSetAttribute((const void*)prep_kernel, hipFuncAttributeMaxDynamicSharedMemorySize, PREP_LDS);
    (void)hipFuncSetAttribute((const void*)main_kernel, hipFuncAttributeMaxDynamicSharedMemorySize, LDS_BYTES);
    PrepArgs pa{(const char*)d_in[0], (const char*)d_in[1], (const char*)d_in[2], (const char*)d_in[4], (const char*)d_in[5], (const char*)d_in[6], ws};
    hipLaunchKernelGGL(prep_kernel, dim3(PREP_UNITS), dim3(256), PREP_LDS, stream, pa);
    MainArgs ma{ws, (char*)d_out};
    hipLaunchKernelGGL(main_kernel, dim3(256), dim3(256), LDS_BYTES, stream, ma);
}
```

```cpp
#define PRETOUCH (2 << 20)
#include <hip/hip_runtime.h>
#include <hip/hip_bf16.h>
#include <type_traits>

#define LAS __attribute__((address_space(3)))
#define GAS __attribute__((address_space(1)))
#define DI __device__ __forceinline__
typedef short bf16x8 __attribute__((ext_vector_type(8)));
typedef short s16x4 __attribute__((ext_vector_type(4)));
typedef float f32x4 __attribute__((ext_vector_type(4)));
typedef float f32x2 __attribute__((ext_vector_type(2)));
typedef unsigned u32x4 __attribute__((ext_vector_type(4)));
typedef unsigned u32x2 __attribute__((ext_vector_type(2)));
typedef __bf16 bf16x2_n __attribute__((ext_vector_type(2)));
typedef GAS unsigned gu32;

constexpr int SEQ = 2048, DM = 640;
constexpr int KSD = 20, KSF = 56, NMT = 128;
constexpr int FRAG = 1024;

constexpr size_t WS_CTL = 0;
constexpr int CTL_LINES = 256;
constexpr int C_TMO = 0, C_KV = 1, C_O = 33, C_X1 = 65, C_HA = 97, C_HB = 129, C_X2 = 161;
constexpr size_t CTL_BYTES = (size_t)CTL_LINES * 256;
constexpr size_t WS_WQKV = CTL_BYTES;
constexpr size_t WS_WO = WS_WQKV + (size_t)16 * 160 * FRAG;
constexpr size_t WS_WGU = WS_WO + (size_t)8 * 100 * FRAG;
constexpr size_t WS_WD = WS_WGU + (size_t)16 * 280 * FRAG;
constexpr size_t WS_CSQ = WS_WD + (size_t)8 * 280 * FRAG;
constexpr size_t WS_CSG = WS_CSQ + (size_t)16 * 128 * 20 * 4;
constexpr size_t XT_BYTES = (size_t)NMT * KSD * FRAG;
constexpr size_t WS_X = WS_CSG + (size_t)16 * 224 * 20 * 4;
constexpr size_t ST_BYTES = (size_t)SEQ * 64;
constexpr size_t WS_ST = WS_X + 4 * XT_BYTES;
constexpr size_t WS_O = WS_ST + 4 * ST_BYTES;
constexpr size_t HT_BYTES = (size_t)NMT * KSF * FRAG;
constexpr size_t WS_H = WS_O + 2 * XT_BYTES;
constexpr size_t KVP_SLOT = 3584;
constexpr size_t WS_KVP = WS_H + 2 * HT_BYTES;
constexpr size_t WS_DUMMY = WS_KVP + (size_t)2 * 16 * 16 * KVP_SLOT;
constexpr size_t WS_END = WS_DUMMY + (size_t)1024 * FRAG;

template <int I, int N, class F> DI void sfor(F&& f) { if constexpr (I < N) { f(std::integral_constant<int, I>{}); sfor<I + 1, N>(f); } }
template <int V> using IC = std::integral_constant<int, V>;

DI unsigned pk_bf16(float lo, float hi) { const bf16x2_n v = __builtin_convertvector((f32x2){lo, hi}, bf16x2_n); return __builtin_bit_cast(unsigned, v); }
DI float bf_lo(unsigned u) { return __uint_as_float(u << 16); }
DI float bf_hi(unsigned u) { return __uint_as_float(u & 0xffff0000u); }
DI float xg_sum(float v) {
    const unsigned u = __float_as_uint(v);
    const auto a = __builtin_amdgcn_permlane16_swap(u, u, false, false);
    const float s = __uint_as_float(a[0]) + __uint_as_float(a[1]);
    const unsigned w = __float_as_uint(s);
    const auto b = __builtin_amdgcn_permlane32_swap(w, w, false, false);
    return __uint_as_float(b[0]) + __uint_as_float(b[1]);
}

template <int N> DI void wait_vm() { static_assert(N >= 0 && N < 64, "vmcnt range"); asm volatile("s_waitcnt vmcnt(%0)" ::"i"(N) : "memory"); }
template <int N> DI void wait_lgkm() { static_assert(N >= 0 && N < 16, "lgkmcnt range"); asm volatile("s_waitcnt lgkmcnt(%0)" ::"i"(N) : "memory"); }
DI void bar() { asm volatile("s_barrier" ::: "memory"); }
template <int OFF> DI void ds_rd(bf16x8& d, unsigned addr) { asm volatile("ds_read_b128 %0, %1 offset:%2" : "=v"(d) : "v"(addr), "i"(OFF) : "memory"); }
template <int T> DI void ds_rd_at(bf16x8& d, unsigned lo, unsigned hi) { ds_rd<T % 65536>(d, T < 65536 ? lo : hi); }
template <int OFF> DI void ld_a(bf16x8& d, const char* sb, unsigned voff) { asm volatile("global_load_dwordx4 %0, %1, %2 offset:%3 sc1" : "=a"(d) : "v"(voff), "s"(sb), "i"(OFF) : "memory"); }
template <int OFF> DI void ld_v(u32x4& d, const char* sb, unsigned voff) { asm volatile("global_load_dwordx4 %0, %1, %2 offset:%3 sc1" : "=v"(d) : "v"(voff), "s"(sb), "i"(OFF) : "memory"); }
DI void ld_v2(u32x2& d, const char* sb, unsigned voff) { asm volatile("global_load_dwordx2 %0, %1, %2 sc1" : "=v"(d) : "v"(voff), "s"(sb) : "memory"); }
template <int OFF = 0> DI void ld_v1(unsigned& d, const char* sb, unsigned voff) { asm volatile("global_load_dword %0, %1, %2 offset:%3 sc1" : "=v"(d) : "v"(voff), "s"(sb), "i"(OFF) : "memory"); }
DI void st_v(const char* sb, unsigned voff, const u32x4& v) { asm volatile("global_store_dwordx4 %0, %1, %2 sc1\n\ts_nop 1" ::"v"(voff), "v"(v), "s"(sb) : "memory"); }
DI void st_v2(const char* sb, unsigned voff, const u32x2& v) { asm volatile("global_store_dwordx2 %0, %1, %2 sc1\n\ts_nop 1" ::"v"(voff), "v"(v), "s"(sb) : "memory"); }
DI void set_m0(unsigned lds) { asm volatile("s_mov_b32 m0, %0\n\ts_nop 0" ::"s"(lds) : "memory"); }
template <int OFF> DI void dma(const char* sb, unsigned voff) { asm volatile("global_load_lds_dwordx4 %0, %1 offset:%2" ::"v"(voff), "s"(sb), "i"(OFF) : "memory"); }
template <int P> DI void dma_piece(const char* sb, unsigned lds, unsigned voff) {
    if constexpr (P % 4 == 0) { set_m0(lds + P * FRAG); }
    dma<(P % 4) * FRAG>(sb + (P / 4) * 4 * FRAG, voff);
}
template <bool FIRST> DI void mma(f32x4& c, const bf16x8& w, const bf16x8& x) {
    if constexpr (FIRST) asm volatile("v_mfma_f32_16x16x32_bf16 %0, %1, %2, 0" : "=v"(c) : "v"(w), "a"(x));
    else asm volatile("v_mfma_f32_16x16x32_bf16 %0, %1, %2, %0" : "+v"(c) : "v"(w), "a"(x));
}
template <bool FIRST> DI void mma_t(f32x4& c, const bf16x8& w, const bf16x8& x) {
    if constexpr (FIRST) asm volatile("v_mfma_f32_16x16x32_bf16 %0, %2, %1, 0" : "=v"(c) : "v"(w), "a"(x));
    else asm volatile("v_mfma_f32_16x16x32_bf16 %0, %2, %1, %0" : "+v"(c) : "v"(w), "a"(x));
}
DI void mma_settle() { asm volatile("s_nop 7\n\ts_nop 7" ::: "memory"); }
DI void mma_fence(f32x4& c) { asm volatile("" : "+v"(c)); }
DI void undef_a(bf16x8& x) { asm volatile("" : "=a"(x)); }
template <class T> DI void pins(T& x) { asm volatile("" : "+s"(x)); }
template <class T> DI void pin(T& x) { asm volatile("" : "+v"(x)); }

DI f32x4 mfma_c(const u32x4& a, const u32x4& b, const f32x4& c) {
    return __builtin_amdgcn_mfma_f32_16x16x32_bf16(__builtin_bit_cast(bf16x8, a), __builtin_bit_cast(bf16x8, b), c, 0, 0, 0);
}
DI float phi(float t) { return (t > 0.f ? t + 1.f : __expf(t)) + 1e-6f; }

__host__ __device__ constexpr int h_tile_base(int f) { return f <= 12 ? 7 * f : 84 + 6 * (f - 12); }
__host__ __device__ constexpr int h_pos_a(int f, int p) { return ((2 * f + p) >> 3) * 14 + ((2 * f + p) & 7); }
__host__ __device__ constexpr int h_pos_b(int ib) { return ib < 12 ? (ib / 6) * 14 + 8 + ib % 6 : (2 + (ib - 12) / 5) * 14 + 8 + (ib - 12) % 5; }

#define STAMP(i)

DI void map16x16(int b, int& r, int& c) { const int x = b & 7, q = b >> 3; r = (x >> 2) * 8 + (q & 7); c = (x & 3) * 4 + (q >> 3); }
DI void map32x8(int b, int& r, int& c) { const int x = b & 7, q = b >> 3; r = (x >> 1) * 8 + (q & 7); c = (x & 1) * 4 + (q >> 3); }

constexpr int PU_X = 32, PU_Q = 40, PU_G = 80, PU_O = 20, PU_D = 54, PREP_UNITS = PU_X + PU_Q + PU_G + PU_O + PU_D;
constexpr int PREP_LDS = 80 * 1024 + 256;
struct PrepArgs { const char *x, *wqkv, *wo, *wg, *wu, *wd; char* ws; const char* out; };

template <int N> DI void tr_frags(bf16x8 (&fr)[N], const unsigned (&addr)[N], unsigned rs) {
    s16x4 lo[N], hi[N];
#pragma unroll
    for (int i = 0; i < N; ++i) asm volatile("ds_read_b64_tr_b16 %0, %2\n\tds_read_b64_tr_b16 %1, %3" : "=&v"(lo[i]), "=&v"(hi[i]) : "v"(addr[i]), "v"(addr[i] + 4 * rs) : "memory");
    asm volatile("s_waitcnt lgkmcnt(0)" ::: "memory");
#pragma unroll
    for (int i = 0; i < N; ++i) { asm volatile("" : "+v"(lo[i]), "+v"(hi[i])); fr[i] = __builtin_shufflevector(lo[i], hi[i], 0, 1, 2, 3, 4, 5, 6, 7); }
}
DI void prep_st16(char* dst, const u32x4& v) { asm volatile("global_store_dwordx4 %0, %1, off sc1\n\ts_nop 1" ::"v"(dst), "v"(v) : "memory"); }
DI void prep_store(char* dst, const bf16x8& fr, int lane) { prep_st16(dst + lane * 16, __builtin_bit_cast(u32x4, fr)); }
DI void prep_colsum(float* dst, const bf16x8& fr, int lane) {
    const bf16x8 ones = {0x3f80, 0x3f80, 0x3f80, 0x3f80, 0x3f80, 0x3f80, 0x3f80, 0x3f80};
    const f32x4 z = {0.f, 0.f, 0.f, 0.f};
    const f32x4 d = __builtin_amdgcn_mfma_f32_16x16x32_bf16(fr, ones, z, 0, 0, 0);
    if ((lane & 15) == 0) prep_st16((char*)(dst + (lane >> 4) * 4), __builtin_bit_cast(u32x4, d));
}
DI void prep_dma(const char* src, LAS char* dst) { __builtin_amdgcn_global_load_lds((const GAS unsigned*)src, (LAS unsigned*)dst, 16, 0, 0); }

__global__ __launch_bounds__(256, 2) void prep_kernel(PrepArgs a) {
    extern __shared__ __attribute__((aligned(16))) char shm[];
    const int tid = threadIdx.x, wid = __builtin_amdgcn_readfirstlane(tid >> 6), lane = tid & 63;
    int u = blockIdx.x;
    const int g = lane >> 4, q = (lane & 15) >> 2, p = lane & 3, c16 = lane & 15;
    LAS char* img = (LAS char*)shm;
    const unsigned imgaddr = (unsigned)(size_t)shm;
    const bf16x8 zero = {0, 0, 0, 0, 0, 0, 0, 0};
#define PSTAMP(x)
#define POUT()
    if (wid == 0) {
        constexpr int NP = (int)((WS_END + PRETOUCH - 1) / PRETOUCH), NO = (SEQ * DM * 2 + PRETOUCH - 1) / PRETOUCH, PER = (NP + NO + 27) / 28;
        const int q = blockIdx.x >> 3;
#pragma unroll
        for (int it = 0; it < (PER + 63) / 64; ++it) {
            const int k = it * 64 + lane, pg = q * PER + k;
            if (k < PER && pg < NP + NO) { const char* p = pg < NP ? a.ws + (size_t)pg * PRETOUCH : a.out + (size_t)(pg - NP) * PRETOUCH;
                __builtin_amdgcn_global_load_lds((const GAS unsigned*)p, (LAS unsigned*)(img + 80 * 1024), 4, 0, 0); }
        }
    }
    if (u < PU_Q) {
        const int ks = u >> 1, hh = u & 1;
#pragma unroll
        for (int it = 0; it < 15; ++it) {
            const int i = (it * 4 + wid) * 64 + lane, row = i / 120, cc = i % 120, seg = cc / 40, c = cc % 40;
            prep_dma(a.wqkv + (size_t)(ks * 32 + row) * 3840 + seg * 1280 + hh * 640 + c * 16, img + (it * 4 + wid) * 1024);
        }
        wait_vm<0>(); __syncthreads(); PSTAMP(pt1);
        const unsigned rs = 1920;
#pragma unroll
        for (int e = 0; e < 2; ++e) {
            const int hl = wid * 2 + e, hp = hh * 8 + hl;
            const unsigned qa = imgaddr + (8 * g + q) * rs + hl * 80, ka = qa + 640, va = qa + 1280;
            const unsigned ad[8] = {ka + 8 * p, ka + 32 + 8 * p, p < 2 ? ka + 64 + 8 * p : va + 64 + 8 * (p - 2), va + 8 * p, va + 32 + 8 * p, qa + 8 * p, qa + 40 + 8 * p, qa + (p == 0 ? 32 : 72)};
            bf16x8 fr[8];
            tr_frags<8>(fr, ad, rs);
            if (c16 >= 8) fr[7] = zero;
            char* w1 = a.ws + WS_WQKV + (size_t)hp * 160 * FRAG + (size_t)ks * 5 * FRAG;
            char* w2 = a.ws + WS_WQKV + (size_t)hp * 160 * FRAG + (size_t)(100 + ks * 3) * FRAG;
            float* cs = (float*)(a.ws + WS_CSQ) + (hp * 20 + ks) * 128;
#pragma unroll
            for (int nt = 0; nt < 8; ++nt) { prep_store(nt < 5 ? w1 + nt * FRAG : w2 + (nt - 5) * FRAG, fr[nt], lane); prep_colsum(cs + nt * 16, fr[nt], lane); }
        }
        PSTAMP(pt2); POUT();
        return;
    }
    u -= PU_Q;
    if (u < PU_X) {
        if (u == 0) __hip_atomic_store((gu32*)(a.ws + WS_CTL + tid * 256), 0u, __ATOMIC_RELAXED, __HIP_MEMORY_SCOPE_AGENT);
        const int mt = u * 4 + wid, r = lane & 15;
        const char* src = a.x + (size_t)mt * 16 * 1280 + lane * 16;
        char* dst = a.ws + WS_X + (size_t)mt * KSD * FRAG + lane * 16;
        LAS char* rows = img + wid * 20 * 1024;
#pragma unroll
        for (int it = 0; it < 20; ++it) prep_dma(src + it * 1024, rows + it * 1024);
        wait_vm<0>();
        u32x4 v[20];
#pragma unroll
        for (int ks = 0; ks < 20; ++ks) v[ks] = *(LAS u32x4*)(rows + r * 1280 + ks * 64 + g * 16);
        const bf16x8 ones = {0x3f80, 0x3f80, 0x3f80, 0x3f80, 0x3f80, 0x3f80, 0x3f80, 0x3f80};
        f32x4 s1 = {0.f, 0.f, 0.f, 0.f}, s2 = {0.f, 0.f, 0.f, 0.f};
#pragma unroll
        for (int ks = 0; ks < 20; ++ks) {
            prep_st16(dst + ks * FRAG, v[ks]);
            const bf16x8 xf = __builtin_bit_cast(bf16x8, v[ks]);
            s1 = __builtin_amdgcn_mfma_f32_16x16x32_bf16(xf, ones, s1, 0, 0, 0);
            s2 = __builtin_amdgcn_mfma_f32_16x16x32_bf16(xf, xf, s2, 0, 0, 0);
        }
        const int j = r & 3;
        const float a1 = j == 0 ? s1[0] : j == 1 ? s1[1] : j == 2 ? s1[2] : s1[3], a2 = j == 0 ? s2[0] : j == 1 ? s2[1] : j == 2 ? s2[2] : s2[3];
        const bool own = (r >> 2) == g;
        const int chunk = (g - (r >> 2)) & 3;
        prep_st16(a.ws + WS_ST + (size_t)(mt * 16 + r) * 64 + chunk * 16, own ? (u32x4){__float_as_uint(a1), __float_as_uint(a2), 0u, 0u} : (u32x4){0u, 0u, 0u, 0u});
        PSTAMP(pt1); PSTAMP(pt2); POUT();
        return;
    }
    u -= PU_X;
    if (u < PU_G) {
        const int ks = u >> 2, fq = u & 3;
#pragma unroll
        for (int it = 0; it < 14; ++it) {
            const int i = (it * 4 + wid) * 64 + lane, row = i / 112, cc = i % 112, up = cc / 56; int c = cc % 56;
            if (fq == 3 && c >= 48) c = 0;
            prep_dma((up ? a.wu : a.wg) + (size_t)(ks * 32 + row) * 3456 + fq * 896 + c * 16, img + (it * 4 + wid) * 1024);
        }
        wait_vm<0>(); __syncthreads(); PSTAMP(pt1);
        const int f = fq * 4 + wid, nt7 = f < 12;
        const unsigned rs = 1792, rowaddr = imgaddr + (8 * g + q) * rs + (h_tile_base(f) - h_tile_base(fq * 4)) * 32;
        unsigned ad[14];
#pragma unroll
        for (int nt = 0; nt < 14; ++nt) { const int j = nt >> 1, up = nt & 1; ad[nt] = rowaddr + up * 896 + 2 * (j < 6 ? 32 * (j >> 1) + 8 * p + 4 * (j & 1) : (nt7 ? 96 : 0) + 4 * p); }
        bf16x8 fr[14];
        tr_frags<14>(fr, ad, rs);
        if (!nt7) { fr[12] = zero; fr[13] = zero; }
        char* da = a.ws + WS_WGU + (size_t)f * 280 * FRAG + (size_t)ks * 8 * FRAG;
        char* db = a.ws + WS_WGU + (size_t)f * 280 * FRAG + (size_t)(160 + ks * 6) * FRAG;
        float* cs = (float*)(a.ws + WS_CSG) + (f * 20 + ks) * 224;
#pragma unroll
        for (int nt = 0; nt < 14; ++nt) { prep_store(nt < 8 ? da + nt * FRAG : db + (nt - 8) * FRAG, fr[nt], lane); prep_colsum(cs + nt * 16, fr[nt], lane); }
        PSTAMP(pt2); POUT();
        return;
    }
    u -= PU_G;
    {
        const bool down = u >= PU_O;
        const int v = down ? u - PU_O : u;
        int kq, s, row_lo, row_hi;
        if (!down) { kq = v / 5; s = v % 5; row_lo = v * 32; row_hi = row_lo + 16; }
        else if (v < 32) { kq = v >> 3; s = v & 7; row_lo = 16 * (h_tile_base(v >> 1) + 2 * (v & 1)); row_hi = row_lo + 16; }
        else {
            const int ib = v - 32, pos = h_pos_b(ib); kq = pos / 14; s = pos % 14;
            if (ib < 16) { row_lo = 16 * (h_tile_base(ib) + 4); row_hi = row_lo + 16; }
            else { const int f0 = 2 * (ib - 16); row_lo = 16 * (h_tile_base(f0) + 6); row_hi = 16 * (h_tile_base(f0 + 1) + 6); }
        }
        const char* w = down ? a.wd : a.wo;
#pragma unroll
        for (int it = 0; it < 10; ++it) {
            const int i = (it * 4 + wid) * 64 + lane, row = i / 80, c = i % 80;
            const int wrow = row < 16 ? row_lo + row : row_hi + row - 16;
            prep_dma(w + (size_t)wrow * 1280 + c * 16, img + (it * 4 + wid) * 1024);
        }
        wait_vm<0>(); __syncthreads(); PSTAMP(pt1);
        const unsigned rs = 1280;
#pragma unroll
        for (int e = 0; e < 2; ++e) {
            const int c8 = wid * 2 + e;
            const unsigned rowaddr = imgaddr + (8 * g + q) * rs + c8 * 160;
            const int pa = (c8 & 1) ? 16 : 0, hf = (c8 & 1) ? 0 : 64;
            unsigned ad[5];
#pragma unroll
            for (int nt = 0; nt < 5; ++nt) ad[nt] = rowaddr + 2 * (nt < 4 ? pa + 32 * (nt >> 1) + 8 * p + 4 * (nt & 1) : hf + 4 * p);
            bf16x8 fr[5];
            tr_frags<5>(fr, ad, rs);
            char* dst = a.ws + (down ? WS_WD + (size_t)((c8 * 4 + kq) * 14 + s) * 5 * FRAG : WS_WO + (size_t)((c8 * 4 + kq) * 5 + s) * 5 * FRAG);
#pragma unroll
            for (int nt = 0; nt < 5; ++nt) prep_store(dst + nt * FRAG, fr[nt], lane);
            if (down && kq >= 2 && s == 12) {
#pragma unroll
                for (int nt = 0; nt < 5; ++nt) prep_store(dst + (5 + nt) * FRAG, zero, lane);
            }
        }
        PSTAMP(pt2); POUT();
    }
}

enum { K_A = 0, K_W = 1, K_X = 2 };
struct Op { int pos, kind, a, b; };
template <int N> struct Sched {
    Op op[N] = {}; int n = 0;
    constexpr void add(int pos, int kind, int a, int b) { op[n] = Op{pos, kind, a, b}; ++n; }
    constexpr void sort() { for (int i = 1; i < n; ++i) { const Op t = op[i]; int j = i - 1; while (j >= 0 && op[j].pos > t.pos) { op[j + 1] = op[j]; --j; } op[j + 1] = t; } }
    constexpr int before(int pos) const { int c = 0; for (int i = 0; i < n; ++i) c += op[i].pos < pos; return c; }
    constexpr int find(int kind, int a, int b) const { for (int i = 0; i < n; ++i) if (op[i].kind == kind && op[i].a == a && op[i].b == b) return i; return -1; }
    constexpr int wait(int pos, int kind, int a, int b) const { const int i = find(kind, a, b); if (i < 0) return 63; const int w = before(pos) - i - 1; return w < 0 ? -1 : (w > 63 ? 63 : w); }
};
constexpr int cmin(int a, int b) { return a < b ? a : b; }

constexpr int NSLOT = 6, SLOT = 20 * FRAG;
constexpr int V_P1 = 0, V_P3 = 12, V_END = 30;
constexpr int slot_of(int v) { return (v % NSLOT) * SLOT; }

enum { X_STAT = 0, X_KVST = 1, X_HST = 2, X_FLAG = 3, X_CS = 4, X_RES = 5, X_WP = 6 };

template <bool FIRST> constexpr Sched<160> make_p1() {
    Sched<160> s;
    s.add(-1, K_X, X_STAT, 0); s.add(-1, K_X, X_STAT, 1);
    if (FIRST) {
        for (int c = 0; c < 6; ++c) {
            for (int p = 0; p < 5; ++p) s.add(-1, K_W, c, p);
            if (c < 4) for (int k = 2 * c; k < 2 * c + 2; ++k) { s.add(-1, K_A, k, 0); s.add(-1, K_A, k, 1); }
        }
        for (int i = 0; i < 40; ++i) s.add(59 + i, K_X, X_CS, i);
        for (int i = 0; i < 3; ++i) s.add(90 + 2 * i, K_X, X_RES, i);
    } else {
        for (int k = 0; k < 8; ++k) { s.add(-1, K_A, k, 0); s.add(-1, K_A, k, 1); }
    }
    for (int k = 8; k < 20; ++k) { s.add(5 * (k - 8), K_A, k, 0); s.add(5 * (k - 8) + 2, K_A, k, 1); }
    for (int c = 0; c < 3; ++c) for (int p = 0; p < 5; ++p) s.add(61 + (c * 5 + p) * 5 / 2, K_W, c < 2 ? 6 + c : V_P3 + 2, p);
    s.sort(); return s;
}
constexpr Sched<128> make_p1b() {
    Sched<128> s;
    s.add(-1, K_X, X_KVST, 0);
    for (int j = 0; j < 25; ++j) s.add(6 + 2 * j, K_X, X_WP, j);
    s.sort(); return s;
}
constexpr Sched<128> make_p2() {
    Sched<128> s;
    for (int k = 0; k < 3; ++k) for (int m = 0; m < 4; ++m) s.add(-1, K_A, k, m);
    for (int k = 3; k < 5; ++k) for (int m = 0; m < 4; ++m) s.add(((k - 3) * 4 + m) * 4 + 1, K_A, k, m);
    const int ch[2] = {V_P3 + 3, V_P3};
    for (int c = 0; c < 2; ++c) for (int p = 0; p < 5; ++p) s.add(40 + (c * 5 + p) * 6, K_W, ch[c], p);
    s.sort(); return s;
}
constexpr int W4_A = 12, W4_B = 57;
constexpr Sched<192> make_p3() {
    Sched<192> s;
    s.add(-1, K_X, X_STAT, 0); s.add(-1, K_X, X_STAT, 1);
    for (int k = 0; k < 6; ++k) { s.add(-1, K_A, k, 0); s.add(-1, K_A, k, 1); }
    for (int k = 6; k < 20; ++k) { s.add(8 * (k - 6) + 1, K_A, k, 0); s.add(8 * (k - 6) + 5, K_A, k, 1); }
    for (int c = 2; c < 10; ++c) for (int p = 0; p < 5; ++p) { int pos = 20 * c - 6 + 1 + p * 4; if (c == 8 && pos < 160) pos = 160; s.add(pos, K_W, V_P3 + c + 4, p); }
    for (int j = 0; j < W4_A; ++j) s.add(113 + 4 * j, K_X, X_WP, j);
    for (int i = 0; i < 4; ++i) s.add(159, K_X, X_HST, i);
    for (int j = W4_A; j < W4_B; ++j) s.add(162 + ((j - W4_A) * 114) / (W4_B - W4_A), K_X, X_WP, j);
    s.add(250, K_X, X_FLAG, 0);
    s.sort(); return s;
}
constexpr int P4_LA = 3;
constexpr Sched<160> make_p4() {
    Sched<160> s;
    for (int i = 0; i < 3; ++i) s.add(-2, K_X, X_HST, i);
    for (int k = 0; k < P4_LA; ++k) for (int m = 0; m < 4; ++m) s.add(-1, K_A, k, m);
    for (int k = P4_LA; k < 14; ++k) for (int m = 0; m < 4; ++m) s.add(20 * (k - P4_LA) + 1 + 4 * m, K_A, k, m);
    for (int j = W4_B; j < 70; ++j) { const int t = j - W4_B; s.add(t < 11 ? 20 * t + 17 : 20 * 11 + 1 + 4 * (t - 11), K_X, X_WP, j); }
    s.add(20 * (8 - P4_LA) - 21, K_X, X_FLAG, 0);
    s.sort(); return s;
}
template <bool FIRST> constexpr Sched<160> SP1 = make_p1<FIRST>();
constexpr auto SP1B = make_p1b();
constexpr auto SP3 = make_p3();
constexpr auto SP2 = make_p2();
constexpr auto SP4 = make_p4();

constexpr int L_RING = 0, L_SCR = NSLOT * SLOT, L_IMG = L_SCR + 8 * FRAG, L_XCH = 4 * SLOT, L_TAB = L_SCR + 32 * FRAG, L_CSQ = L_TAB + 1024, L_CSG = L_CSQ + 512, L_STAMP = L_CSG + 1024,
              LDS_BYTES = L_STAMP + 512;
struct MainArgs { char* ws; char* out; };

DI void row_stats(const u32x4& p, float& rstd, float& mrs) {
    float s = __uint_as_float(p[0]) + __uint_as_float(p[2]), s2 = __uint_as_float(p[1]) + __uint_as_float(p[3]);
    s = xg_sum(s); s2 = xg_sum(s2);
    const float mean = s * (1.f / DM), var = fmaxf(s2 * (1.f / DM) - mean * mean, 0.f);
    rstd = __builtin_amdgcn_rsqf(var + 1e-5f);
    mrs = mean * rstd;
}
DI void spin_ge(const char* ws, int line, unsigned target) {
    gu32* p = (gu32*)(ws + WS_CTL + (size_t)line * 256);
    gu32* tmo = (gu32*)(ws + WS_CTL + C_TMO * 256);
    unsigned spins = 0;
    while ((unsigned)__builtin_amdgcn_readfirstlane(__hip_atomic_load(p, __ATOMIC_RELAXED, __HIP_MEMORY_SCOPE_AGENT)) < target) {
        __builtin_amdgcn_s_sleep(1);
        ++spins;
        if ((spins & 1023u) == 0u && __builtin_amdgcn_readfirstlane(__hip_atomic_load(tmo, __ATOMIC_RELAXED, __HIP_MEMORY_SCOPE_AGENT))) break;
        if (spins > (1u << 17)) { __hip_atomic_store(tmo, 1u, __ATOMIC_RELAXED, __HIP_MEMORY_SCOPE_AGENT); break; }
    }
    asm volatile("" ::: "memory");
}
DI void signal(char* ws, int line) { __hip_atomic_fetch_add((gu32*)(ws + WS_CTL + (size_t)line * 256), 1u, __ATOMIC_RELAXED, __HIP_MEMORY_SCOPE_AGENT); }
template <bool FIRST> DI void mma_av(f32x4& c, const bf16x8& w, const bf16x8& x) {
    if constexpr (FIRST) asm volatile("v_mfma_f32_16x16x32_bf16 %0, %1, %2, 0" : "=v"(c) : "a"(w), "v"(x));
    else asm volatile("v_mfma_f32_16x16x32_bf16 %0, %1, %2, %0" : "+v"(c) : "a"(w), "v"(x));
}
template <int OFF> DI void ld_x(bf16x8& d, const char* sb, unsigned voff) { asm volatile("global_load_dwordx4 %0, %1, %2 offset:%3 sc1" : "=v"(d) : "v"(voff), "s"(sb), "i"(OFF) : "memory"); }
template <int OFF> DI void ds_wr(unsigned addr, const f32x4& v) { asm volatile("ds_write_b128 %0, %1 offset:%2" ::"v"(addr), "v"(v), "i"(OFF) : "memory"); }
template <int OFF> DI void ld_w(bf16x8& d, const char* sb, unsigned voff) { asm volatile("global_load_dwordx4 %0, %1, %2 offset:%3" : "=a"(d) : "v"(voff), "s"(sb), "i"(OFF) : "memory"); }

__global__ __launch_bounds__(256, 1) void main_kernel(MainArgs a) {
    extern __shared__ __attribute__((aligned(16))) char shm[];
    const unsigned lds0 = (unsigned)(size_t)shm;
    LAS char* lds = (LAS char*)shm;
    char* const ws = a.ws;
    const int tid = threadIdx.x, wid = __builtin_amdgcn_readfirstlane(tid >> 6), lane = tid & 63, g = lane >> 4, r = lane & 15;
    const unsigned voff = lane * 16;
    int rp, cg; map16x16(blockIdx.x, rp, cg);
    int rt, c8; map32x8(blockIdx.x, rt, c8);
    const int mt_a = rp * 8 + wid * 2;
    const int mtf = rt * 4 + wid, odd = c8 & 1;
    const int tA = odd ? (5 * c8 + 1) / 2 : 5 * c8 / 2, tH = odd ? (5 * c8 - 1) / 2 : 5 * c8 / 2 + 2;
    const unsigned hoff = tH * FRAG + (odd * 2 + (g >> 1)) * 256 + r * 16 + (g & 1) * 8;
    STAMP(0);

    const char* wP1 = ws + WS_WQKV + (size_t)(cg * 160 + wid * 5) * FRAG;
    const char* wP3 = ws + WS_WGU + (size_t)(cg * 280 + wid * 5) * FRAG;
    const char* wP2 = ws + WS_WO + (size_t)((c8 * 4 + wid) * 25) * FRAG;
    const char* wP4 = ws + WS_WD + (size_t)((c8 * 4 + wid) * 70) * FRAG;
    unsigned wdst = lds0 + L_RING + wid * 5 * FRAG;
    auto fresh_ptrs = [&]() { pins(wP1); pins(wP2); pins(wP3); pins(wP4); pins(wdst); };
    auto w_op = [&](auto V, auto P) {
        constexpr int v0 = V, v = v0 % V_END, p = P;
        const char* src = v < V_P3 ? wP1 + (size_t)(v - V_P1) * SLOT : wP3 + (size_t)(v - V_P3) * SLOT;
        dma_piece<p>(src, wdst + slot_of(v), voff);
    };
    auto w_chunk = [&](auto V) { sfor<0, 5>([&](auto P) { w_op(V, P); }); };
    const unsigned rdb = lds0 + L_RING + voff, rdb_hi = rdb + 65536;

    u32x4 resA, resB; u32x2 resH;
    float csk[5]; f32x4 csq[3];
    bf16x8 W2[25], W4[70];

    for (int l = 0; l < 2; ++l) {
        {
            const char* xsrc = ws + WS_X + (size_t)(2 * l) * XT_BYTES + (size_t)mt_a * KSD * FRAG;
            const char* stsrc = ws + WS_ST + (size_t)(2 * l) * ST_BYTES + (size_t)mt_a * 16 * 64;
            char* kvp = ws + WS_KVP + (size_t)((l * 16 + cg) * 16) * KVP_SLOT;
            if (l > 0) { if (wid == 0) spin_ge(ws, C_X2 + rp, 16u); wait_vm<0>(); bar(); }
            STAMP(l * 24 + 1);
            fresh_ptrs(); pins(xsrc);
            bf16x8 A[2][20];
            f32x4 acc1[2][5], acc2[2][3];
            float rstd[2], mrs[2];
            f32x4 st[2][2];
            auto pass1 = [&](auto FST) {
                constexpr bool first = FST;
                u32x4 stp[2]; unsigned cq[20], cgp[20];
                const char* csqb = ws + WS_CSQ + (size_t)cg * 20 * 512; const char* csgb = ws + WS_CSG + (size_t)cg * 20 * 896; const char* xr = ws + WS_X + (size_t)mtf * KSD * FRAG;
                pins(csqb); pins(csgb); pins(xr);
                auto emit = [&](auto K) {
                    constexpr Op o = SP1<first>.op[K];
                    if constexpr (o.kind == K_A) ld_a<(o.a % 4) * FRAG>(A[o.b][o.a], xsrc + (size_t)(o.b * KSD + (o.a / 4) * 4) * FRAG, voff);
                    else if constexpr (o.kind == K_W) w_op(IC<o.a>{}, IC<o.b>{});
                    else if constexpr (o.a == X_STAT) ld_v<o.b * FRAG>(stp[o.b], stsrc, r * 64 + g * 16);
                    else if constexpr (o.a == X_CS) {
                        if constexpr (o.b < 20) ld_v1<(o.b % 8) * 512>(cq[o.b], csqb + (o.b / 8) * 4096, (tid & 127) * 4);
                        else ld_v1<((o.b - 20) % 4) * 896>(cgp[o.b - 20], csgb + ((o.b - 20) / 4) * 3584, (tid < 224 ? tid : 0) * 4);
                    } else if constexpr (o.a == X_RES) {
                        if constexpr (o.b == 0) ld_v<0>(resA, xr + tA * FRAG, voff); else if constexpr (o.b == 1) ld_v<FRAG>(resB, xr + tA * FRAG, voff); else ld_v2(resH, xr, hoff);
                    }
                };
                sfor<0, SP1<first>.before(0)>(emit);
                wait_vm<SP1<first>.wait(0, K_X, X_STAT, 1)>();
                pin(stp[0]); pin(stp[1]);
                row_stats(stp[0], rstd[0], mrs[0]); row_stats(stp[1], rstd[1], mrs[1]);
                if (g == 0) {
                    *(LAS f32x2*)(lds + L_TAB + (wid * 32 + r) * 8) = (f32x2){rstd[0], mrs[0]};
                    *(LAS f32x2*)(lds + L_TAB + (wid * 32 + 16 + r) * 8) = (f32x2){rstd[1], mrs[1]};
                }
                sfor<0, 2>([&](auto M) { st[M][0] = *(LAS f32x4*)(lds + L_TAB + (wid * 32 + M * 16 + 4 * g) * 8); st[M][1] = *(LAS f32x4*)(lds + L_TAB + (wid * 32 + M * 16 + 4 * g) * 8 + 16); });
                pin(st[0][0]); pin(st[0][1]); pin(st[1][0]); pin(st[1][1]);
                constexpr int L = 4, R = 5;
                bf16x8 B[R];
                auto rd = [&](auto I) { constexpr int i = I; if constexpr (i < 100) ds_rd_at<slot_of(V_P1 + i / 20) + (i % 20) * FRAG>(B[i % R], rdb, rdb_hi); };
                if constexpr (first) { wait_vm<SP1<first>.wait(0, K_W, 0, 4)>(); bar(); }
                sfor<0, L>(rd);
                wait_lgkm<L - 1>();
                sfor<0, 100>([&](auto I) {
                    constexpr int i = I, ks = i / 5, n = i % 5;
                    if constexpr (n == 0) wait_vm<SP1<first>.wait(i, K_A, ks, 1)>();
                    if constexpr (first && i % 20 == 20 - L && i < 80) { wait_vm<SP1<first>.wait(i, K_W, i / 20 + 1, 4)>(); bar(); }
                    if constexpr (i == 60) bar();
                    mma_t<ks == 0>(acc1[0][n], B[i % R], A[0][ks]);
                    rd(IC<i + L>{});
                    mma_t<ks == 0>(acc1[1][n], B[i % R], A[1][ks]);
                    sfor<SP1<first>.before(i), SP1<first>.before(i + 1)>(emit);
                    if constexpr (i + 1 < 100) wait_lgkm<cmin(L - 1, 98 - i)>();
                });
                if constexpr (first) {
                    wait_vm<cmin(SP1<first>.n - SP1<first>.find(K_X, X_RES, 2) - 1, SP1<first>.n - SP1<first>.find(K_X, X_CS, 39) - 1)>();
                    float sq = 0.f, sg = 0.f;
                    sfor<0, 20>([&](auto J) { constexpr int j = J; pin(cq[j]); pin(cgp[j]); sq += __uint_as_float(cq[j]); sg += __uint_as_float(cgp[j]); });
                    pin(resA); pin(resB); pin(resH);
                    if (tid < 128) *(LAS float*)(lds + L_CSQ + tid * 4) = sq;
                    if (tid < 224) *(LAS float*)(lds + L_CSG + tid * 4) = sg;
                    wait_lgkm<0>(); bar();
                    sfor<0, 5>([&](auto N) { csk[N] = *(LAS float*)(lds + L_CSQ + (N * 16 + r) * 4); });
                    sfor<0, 3>([&](auto N) { csq[N] = *(LAS f32x4*)(lds + L_CSQ + ((5 + N) * 16 + 4 * g) * 4); });
                }
            };
            if (l == 0) pass1(std::true_type{}); else pass1(std::false_type{});
            mma_settle();
            sfor<0, 5>([&](auto N) { mma_fence(acc1[0][N]); mma_fence(acc1[1][N]); });
            STAMP(l * 24 + 2);
            {
                u32x4 op[5];
                sfor<0, 5>([&](auto N) {
                    constexpr int n = N;
                    float v[2][4];
                    sfor<0, 2>([&](auto M) {
                        sfor<0, 4>([&](auto J) { constexpr int m = M, j = J;
                            float t = acc1[m][n][j] * st[m][j >> 1][(j & 1) * 2] - st[m][j >> 1][(j & 1) * 2 + 1] * csk[n];
                            if constexpr (n < 2) t = phi(t); else if constexpr (n == 2) t = r < 8 ? phi(t) : t;
                            v[m][j] = t; });
                    });
                    op[n] = (u32x4){pk_bf16(v[0][0], v[0][1]), pk_bf16(v[0][2], v[0][3]), pk_bf16(v[1][0], v[1][1]), pk_bf16(v[1][2], v[1][3])};
                });
                u32x4 v2 = op[2];
                if (r < 8) v2 = (u32x4){0x3f803f80u, 0x3f803f80u, 0x3f803f80u, 0x3f803f80u};
                const f32x4 z4 = {0.f, 0.f, 0.f, 0.f};
                f32x4 kv[8];
                kv[0] = mfma_c(op[0], op[3], z4); kv[1] = mfma_c(op[0], op[4], z4); kv[2] = mfma_c(op[0], v2, z4);
                kv[3] = mfma_c(op[1], op[3], z4); kv[4] = mfma_c(op[1], op[4], z4); kv[5] = mfma_c(op[1], v2, z4);
                kv[6] = mfma_c(op[2], op[4], z4); kv[7] = mfma_c(op[2], v2, z4);
                sfor<0, 8>([&](auto T) { *(LAS f32x4*)(lds + L_SCR + (wid * 8 + T) * FRAG + voff) = kv[T]; });
            }
            wait_lgkm<0>(); bar();
            int s_dg, s_e; bool s_z;
            { const int t = tid < 210 ? tid : 209; s_z = t >= 200; const int hd = t / 100, v = t % 100; s_dg = s_z ? t - 200 : hd * 5 + v / 20; s_e = s_z ? 0 : hd * 20 + v % 20; }
            const unsigned kvo = (tid < 210 ? tid : 209) * 16;
            {
                const int dt = s_dg >> 2, gg = s_dg & 3;
                const int et = s_z ? 2 : s_e >> 4, cc = s_z ? 0 : (s_e < 32 ? s_e & 15 : s_e - 24);
                const int T = dt == 2 ? 5 + et : dt * 3 + et;
                const unsigned o = L_SCR + T * FRAG + (gg * 16 + cc) * 16;
                const f32x4 sum = (*(LAS f32x4*)(lds + o) + *(LAS f32x4*)(lds + o + 8 * FRAG)) + (*(LAS f32x4*)(lds + o + 16 * FRAG) + *(LAS f32x4*)(lds + o + 24 * FRAG));
                st_v(kvp + rp * KVP_SLOT, kvo, __builtin_bit_cast(u32x4, sum));
            }
            STAMP(l * 24 + 3);
            fresh_ptrs();
            {
                constexpr int R2 = 6, L2 = 4;
                bf16x8 B2[R2];
                auto rd = [&](auto I) { constexpr int i = I; if constexpr (i < 60) ds_rd_at<slot_of(V_P1 + 5 + i / 20) + (i % 20) * FRAG>(B2[i % R2], rdb, rdb_hi); };
                auto emit = [&](auto K) { constexpr Op o = SP1B.op[K]; if constexpr (o.a == X_WP) ld_w<(o.b % 4) * FRAG>(W2[o.b], wP2 + (size_t)(o.b / 4) * 4 * FRAG, voff); };
                sfor<0, L2>(rd);
                wait_lgkm<L2 - 1>();
                sfor<0, 60>([&](auto I) {
                    constexpr int i = I, ks = i / 3, n = i % 3;
                    if constexpr (i == 20 - L2) {
                        wait_vm<SP1B.wait(i, K_X, X_KVST, 0)>(); bar();
                        if (tid == 0) signal(ws, C_KV + l * 16 + cg);
                        *(LAS u32x4*)(lds + L_IMG + tid * 16) = (u32x4){0u, 0u, 0u, 0u};
                        if (tid < 128) *(LAS u32x4*)(lds + L_IMG + 4096 + tid * 16) = (u32x4){0u, 0u, 0u, 0u};
                    }
                    mma<ks == 0>(acc2[0][n], B2[i % R2], A[0][ks]);
                    rd(IC<i + L2>{});
                    mma<ks == 0>(acc2[1][n], B2[i % R2], A[1][ks]);
                    sfor<SP1B.before(i), SP1B.before(i + 1)>(emit);
                    if constexpr (i + 1 < 60) wait_lgkm<cmin(L2 - 1, 58 - i)>();
                });
            }
            mma_settle();
            sfor<0, 3>([&](auto N) { mma_fence(acc2[0][N]); mma_fence(acc2[1][N]); });
            STAMP(l * 24 + 4);
            if (wid == 0) spin_ge(ws, C_KV + l * 16 + cg, 16u);
            wait_lgkm<0>(); bar();
            STAMP(l * 24 + 5);
            u32x4 pv[16];
            sfor<0, 16>([&](auto I) { ld_v<0>(pv[I], kvp + I * KVP_SLOT, kvo); });
            u32x4 qa[2], qb[2];
            sfor<0, 2>([&](auto M) {
                constexpr int m = M;
                unsigned pk[3][2];
                sfor<0, 3>([&](auto N) {
                    constexpr int n = N;
                    float v[4];
                    sfor<0, 4>([&](auto J) { constexpr int j = J; v[j] = phi(acc2[m][n][j] * rstd[m] - mrs[m] * csq[n][j]); });
                    pk[n][0] = pk_bf16(v[0], v[1]); pk[n][1] = pk_bf16(v[2], v[3]);
                });
                qa[m] = (u32x4){pk[0][0], pk[0][1], pk[2][0], pk[2][1]};
                qb[m] = (u32x4){pk[1][0], pk[1][1], pk[2][0], pk[2][1]};
            });
            pin(qa[0]); pin(qa[1]); pin(qb[0]); pin(qb[1]);
            {
                wait_vm<0>();
                sfor<0, 16>([&](auto I) { pin(pv[I]); });
                f32x4 s0 = __builtin_bit_cast(f32x4, pv[0]) + __builtin_bit_cast(f32x4, pv[1]), s1 = __builtin_bit_cast(f32x4, pv[2]) + __builtin_bit_cast(f32x4, pv[3]);
#pragma unroll
                for (int i = 4; i < 16; i += 2) { s0 += __builtin_bit_cast(f32x4, pv[i]); s1 += __builtin_bit_cast(f32x4, pv[i + 1]); }
                const f32x4 s = s0 + s1;
                const u32x2 w = {pk_bf16(s[0], s[1]), pk_bf16(s[2], s[3])};
                const int hd = s_dg >= 5, dl = s_dg - hd * 5;
                const int gg = dl < 4 ? dl : hd, hb = dl < 4 ? 0 : 8;
                if (tid < 200) {
                    const int e = s_e, T = e >= 32 ? 2 : (e >> 2) & 1, c = e >= 32 ? e - 32 : (e >> 3) * 4 + (e & 3);
                    const int img = T == 2 ? 4 : T * 2 + hd;
                    *(LAS u32x2*)(lds + L_IMG + img * FRAG + (gg * 16 + c) * 16 + hb) = w;
                } else if (tid < 210) {
                    *(LAS u32x2*)(lds + L_IMG + 5 * FRAG + hd * 64 + gg * 16 + hb) = w;
                }
            }
            wait_lgkm<0>(); bar();
            STAMP(l * 24 + 6);
            {
                u32x4 im[5];
                sfor<0, 5>([&](auto T) { im[T] = *(LAS u32x4*)(lds + L_IMG + T * FRAG + voff); });
                const u32x4 da = *(LAS u32x4*)(lds + L_IMG + 5 * FRAG + g * 16), db = *(LAS u32x4*)(lds + L_IMG + 5 * FRAG + 64 + g * 16);
                const f32x4 z4 = {0.f, 0.f, 0.f, 0.f};
                const char* obase = ws + WS_O + (size_t)l * XT_BYTES + (size_t)mt_a * KSD * FRAG;
                u32x2 t2p[2];
                sfor<0, 2>([&](auto M) {
                    constexpr int m = M;
                    f32x4 t0 = mfma_c(im[0], qa[m], z4); t0 = mfma_c(im[1], qb[m], t0);
                    f32x4 t1 = mfma_c(im[2], qa[m], z4); t1 = mfma_c(im[3], qb[m], t1);
                    const f32x4 t2 = mfma_c(im[4], qb[m], z4);
                    const f32x4 dA = mfma_c(da, qa[m], z4), dB = mfma_c(db, qb[m], z4);
                    const float ia = 1.f / (dA[0] + 1e-6f), ib = 1.f / (dB[0] + 1e-6f);
                    const float i0 = g < 3 ? ia : ib, i1 = g < 2 ? ia : ib;
                    const u32x4 w = {pk_bf16(t0[0] * i0, t0[1] * i0), pk_bf16(t0[2] * i0, t0[3] * i0), pk_bf16(t1[0] * i1, t1[1] * i1), pk_bf16(t1[2] * i1, t1[3] * i1)};
                    const int G = 5 * cg + g;
                    st_v(obase, (m * KSD + (G >> 2)) * FRAG + (G & 3) * 256 + r * 16, w);
                    t2p[m] = (u32x2){pk_bf16(t2[0] * ib, t2[1] * ib), pk_bf16(t2[2] * ib, t2[3] * ib)};
                });
                const auto s0 = __builtin_amdgcn_permlane16_swap(t2p[0][0], t2p[1][0], false, false);
                const auto s1 = __builtin_amdgcn_permlane16_swap(t2p[0][1], t2p[1][1], false, false);
                const u32x4 w = {s0[0], s1[0], s0[1], s1[1]};
                const int G = 5 * cg + 4;
                if (g < 2) st_v(obase, (g * KSD + (G >> 2)) * FRAG + (G & 3) * 256 + r * 16, w);
            }
            wait_vm<0>(); bar();
            if (tid == 0) signal(ws, C_O + l * 16 + rp);
            STAMP(l * 24 + 7);
        }
        auto proj = [&](auto PH) {
            constexpr int ph = PH;
            constexpr int NS = ph == 2 ? 5 : 14, KS = ph == 2 ? KSD : KSF;
            const char* abase = ws + (ph == 2 ? WS_O + (size_t)l * XT_BYTES : WS_H + (size_t)l * HT_BYTES) + (size_t)(rt * 4 * KS + wid * NS) * FRAG;
            const char* asrc[4]; const char* alast[4];
            sfor<0, 4>([&](auto M) { asrc[M] = abase + (size_t)(((wid + M) & 3) * KS) * FRAG; pins(asrc[M]);
                alast[M] = asrc[M] + (size_t)((ph == 4 && wid >= 2) ? 12 : NS - 1) * FRAG; });
            fresh_ptrs();
            constexpr int AW = ph == 2 ? 5 : P4_LA + 1;
            bf16x8 A[AW][4];
            f32x4 acc[4][5];
            unsigned flagv;
            auto emit = [&](auto K) {
                constexpr Op o = ph == 2 ? SP2.op[K] : SP4.op[K];
                if constexpr (o.kind == K_A) {
                    if constexpr (o.a == NS - 1) ld_x<0>(A[o.a % AW][o.b], alast[o.b], voff);
                    else ld_x<(o.a % 4) * FRAG>(A[o.a % AW][o.b], asrc[o.b] + (size_t)(o.a / 4) * 4 * FRAG, voff);
                }
                else if constexpr (o.kind == K_W) w_op(IC<o.a>{}, IC<o.b>{});
                else if constexpr (o.a == X_WP) ld_w<(o.b % 4) * FRAG>(W4[o.b], wP4 + (size_t)(o.b / 4) * 4 * FRAG, voff);
                else if constexpr (o.a == X_FLAG) ld_v1(flagv, ws + WS_CTL + (size_t)(C_HB + l * 16 + (rt >> 1)) * 256, 0u);
            };
            constexpr int first = ph == 2 ? 0 : 3;
            constexpr int npro = ph == 2 ? SP2.before(0) : SP4.before(0);
            sfor<first, npro>(emit);
            unsigned xw[3];
            sfor<0, 3>([&](auto M) { xw[M] = lds0 + L_XCH + ((((wid + M + 1) & 3) * 3 + M) * 5) * FRAG + voff; });
            sfor<0, NS * 20>([&](auto I) {
                constexpr int i = I, s = i / 20;
                constexpr bool last = s == NS - 1;
                constexpr int n = last ? i % 5 : (i % 20) / 4, m = last ? (((i % 20) / 5 + 1) & 3) : i % 4;
                if constexpr (i % 20 == 0) {
                    constexpr int wa = ph == 2 ? SP2.wait(i, K_A, s, 3) : SP4.wait(i, K_A, s, 3);
                    constexpr int ww = ph == 2 ? 63 : SP4.wait(i, K_X, X_WP, s * 5 + 4);
                    wait_vm<cmin(wa, ww)>();
                    if constexpr (ph == 4 && i == 0) { bar(); if (tid == 0) signal(ws, C_HB + l * 16 + rp); }
                }
                if constexpr (ph == 4 && i == 20 * (8 - P4_LA)) {
                    wait_vm<SP4.wait(i, K_X, X_FLAG, 0)>();
                    pin(flagv);
                    if (__builtin_amdgcn_readfirstlane(flagv) < 16) spin_ge(ws, C_HB + l * 16 + (rt >> 1), 16u);
                }
                if constexpr (ph == 2) mma_av<s == 0>(acc[m][n], W2[s * 5 + n], A[s % AW][m]);
                else mma_av<s == 0>(acc[m][n], W4[s * 5 + n], A[s % AW][m]);
                if constexpr (ph == 2) sfor<SP2.before(i), SP2.before(i + 1)>(emit); else sfor<SP4.before(i), SP4.before(i + 1)>(emit);
                if constexpr (last && i % 20 >= 10) { constexpr int q = i % 20 - 10, t = q / 5 + 1; ds_wr<(q % 5) * FRAG>(xw[t - 1], acc[t][q % 5]); }
            });
            mma_settle();
            sfor<0, 4>([&](auto M) { sfor<0, 5>([&](auto N) { mma_fence(acc[M][N]); }); });
            STAMP(l * 24 + (ph == 2 ? 9 : 17));
            sfor<0, 5>([&](auto N) { ds_wr<N * FRAG>(xw[2], acc[3][N]); });
            wait_lgkm<0>(); bar();
            f32x4 fin[5];
            sfor<0, 5>([&](auto N) {
                const unsigned o = L_XCH + (wid * 15 + N) * FRAG + voff;
                fin[N] = (acc[0][N] + *(LAS f32x4*)(lds + o)) + (*(LAS f32x4*)(lds + o + 5 * FRAG) + *(LAS f32x4*)(lds + o + 10 * FRAG));
            });
            auto fin_pair = [&](const f32x4& lo, const f32x4& hi, const u32x4& x) {
                u32x4 w;
                w[0] = pk_bf16(lo[0] + bf_lo(x[0]), lo[1] + bf_hi(x[0])); w[1] = pk_bf16(lo[2] + bf_lo(x[1]), lo[3] + bf_hi(x[1]));
                w[2] = pk_bf16(hi[0] + bf_lo(x[2]), hi[1] + bf_hi(x[2])); w[3] = pk_bf16(hi[2] + bf_lo(x[3]), hi[3] + bf_hi(x[3]));
                return w;
            };
            resA = fin_pair(fin[0], fin[1], resA); resB = fin_pair(fin[2], fin[3], resB);
            resH = (u32x2){pk_bf16(fin[4][0] + bf_lo(resH[0]), fin[4][1] + bf_hi(resH[0])), pk_bf16(fin[4][2] + bf_lo(resH[1]), fin[4][3] + bf_hi(resH[1]))};
            if (ph == 4 && l == 1) {
                char* row = a.out + (size_t)(mtf * 16 + r) * 1280;
                *(GAS u32x4*)(row + tA * 64 + g * 16) = resA;
                *(GAS u32x4*)(row + tA * 64 + 64 + g * 16) = resB;
                *(GAS u32x2*)(row + tH * 64 + odd * 32 + g * 8) = resH;
            } else {
                const int xo = 2 * l + (ph == 2 ? 1 : 2);
                const char* xb = ws + WS_X + (size_t)xo * XT_BYTES + (size_t)mtf * KSD * FRAG;
                st_v(xb + tA * FRAG, voff, resA); st_v(xb + (tA + 1) * FRAG, voff, resB); st_v2(xb, hoff, resH);
                float s1 = 0.f, s2 = 0.f;
                auto accum = [&](unsigned u) { const float lo = bf_lo(u), hi = bf_hi(u); s1 += lo + hi; s2 += lo * lo + hi * hi; };
                sfor<0, 4>([&](auto J) { constexpr int j = J; accum(resA[j]); accum(resB[j]); });
                accum(resH[0]); accum(resH[1]);
                s1 = xg_sum(s1); s2 = xg_sum(s2);
                if (g == 0) st_v2(ws + WS_ST + (size_t)xo * ST_BYTES + (size_t)mtf * 16 * 64, r * 64 + c8 * 8, (u32x2){__float_as_uint(s1), __float_as_uint(s2)});
                wait_vm<0>(); bar();
                if (tid == 0) signal(ws, (ph == 2 ? C_X1 + l * 16 : C_X2) + (rt >> 1));
                fresh_ptrs();
                if constexpr (ph == 2) { w_chunk(IC<V_P3 + 1>{}); w_chunk(IC<V_P3 + 4>{}); w_chunk(IC<V_P3 + 5>{}); }
                else sfor<0, 6>([&](auto V) { w_chunk(IC<V_END + V>{}); });
            }
        };
        if (wid == 0) spin_ge(ws, C_O + l * 16 + (rt >> 1), 16u);
        bar();
        STAMP(l * 24 + 8);
        proj(IC<2>{});
        STAMP(l * 24 + 10);
        {
            const char* xsrc = ws + WS_X + (size_t)(2 * l + 1) * XT_BYTES + (size_t)mt_a * KSD * FRAG;
            const char* stsrc = ws + WS_ST + (size_t)(2 * l + 1) * ST_BYTES + (size_t)mt_a * 16 * 64;
            const char* hb = ws + WS_H + (size_t)l * HT_BYTES + (size_t)mt_a * KSF * FRAG;
            if (wid == 0) spin_ge(ws, C_X1 + l * 16 + rp, 16u);
            wait_vm<0>(); bar();
            STAMP(l * 24 + 11);
            fresh_ptrs(); pins(xsrc);
            bf16x8 A[2][20];
            f32x4 accA[2][8], accB[2][6];
            u32x4 stp[2]; unsigned flagv;
            auto emit = [&](auto K) {
                constexpr Op o = SP3.op[K];
                if constexpr (o.kind == K_A) ld_a<(o.a % 4) * FRAG>(A[o.b][o.a], xsrc + (size_t)(o.b * KSD + (o.a / 4) * 4) * FRAG, voff);
                else if constexpr (o.kind == K_W) w_op(IC<o.a>{}, IC<o.b>{});
                else if constexpr (o.a == X_STAT) ld_v<o.b * FRAG>(stp[o.b], stsrc, r * 64 + g * 16);
                else if constexpr (o.a == X_WP) ld_w<(o.b % 4) * FRAG>(W4[o.b], wP4 + (size_t)(o.b / 4) * 4 * FRAG, voff);
                else if constexpr (o.a == X_FLAG) ld_v1(flagv, ws + WS_CTL + (size_t)(C_HA + l * 16 + (rt >> 1)) * 256, 0u);
            };
            sfor<0, SP3.before(0)>(emit);
            float rstd[2], mrs[2];
            wait_vm<SP3.wait(0, K_X, X_STAT, 1)>();
            pin(stp[0]); pin(stp[1]);
            row_stats(stp[0], rstd[0], mrs[0]); row_stats(stp[1], rstd[1], mrs[1]);
            pin(rstd[0]); pin(rstd[1]); pin(mrs[0]); pin(mrs[1]);
            constexpr int L = 6, R = 7;
            bf16x8 B[R];
            f32x4 cs[8];
            auto rd = [&](auto I, auto LIM) { constexpr int i = I; if constexpr (i < LIM) ds_rd_at<slot_of(V_P3 + i / 20) + (i % 20) * FRAG>(B[i % R], rdb, rdb_hi); };
            sfor<0, L>([&](auto I) { rd(I, IC<160>{}); });
            wait_lgkm<L - 1>();
            sfor<0, 160>([&](auto I) {
                constexpr int i = I, ks = i / 8, n = i % 8;
                if constexpr (n == 0) wait_vm<SP3.wait(i, K_A, ks, 1)>();
                if constexpr (i % 20 == 20 - L && i / 20 + 1 >= 2 && i / 20 + 1 < 8) { wait_vm<SP3.wait(i, K_W, V_P3 + i / 20 + 1, 4)>(); bar(); }
                if constexpr (i == 154) sfor<0, 8>([&](auto N) { cs[N] = *(LAS f32x4*)(lds + L_CSG + (N * 16 + 4 * g) * 4); });
                mma<ks == 0>(accA[0][n], B[i % R], A[0][ks]);
                rd(IC<i + L>{}, IC<160>{});
                mma<ks == 0>(accA[1][n], B[i % R], A[1][ks]);
                sfor<SP3.before(i), (i == 159 ? SP3.find(K_X, X_HST, 0) : SP3.before(i + 1))>(emit);
                if constexpr (i + 1 < 160) wait_lgkm<cmin(L - 1, 158 - i)>();
            });
            mma_settle();
            sfor<0, 8>([&](auto N) { mma_fence(accA[0][N]); mma_fence(accA[1][N]); });
            STAMP(l * 24 + 12);
            auto h_tile = [&](const f32x4& ga, const f32x4& ua, const f32x4& cgv, const f32x4& cuv, int m) {
                const f32x4 h = (ga * rstd[m] - cgv * mrs[m]) * (ua * rstd[m] - cuv * mrs[m]);
                return (u32x2){pk_bf16(h[0], h[1]), pk_bf16(h[2], h[3])};
            };
            sfor<0, 2>([&](auto P) {
                sfor<0, 2>([&](auto M) {
                    constexpr int p = P, m = M;
                    const u32x2 lo = h_tile(accA[m][4 * p], accA[m][4 * p + 1], cs[4 * p], cs[4 * p + 1], m), hi = h_tile(accA[m][4 * p + 2], accA[m][4 * p + 3], cs[4 * p + 2], cs[4 * p + 3], m);
                    st_v(hb + (size_t)(m * KSF + h_pos_a(cg, p)) * FRAG, voff, (u32x4){lo[0], lo[1], hi[0], hi[1]});
                });
            });
            STAMP(l * 24 + 13);
            fresh_ptrs();
            wait_vm<SP3.wait(160, K_W, V_P3 + 8, 4)>(); wait_lgkm<0>(); bar();
            sfor<160, 160 + L>([&](auto I) { rd(I, IC<280>{}); });
            wait_lgkm<L - 1>();
            sfor<160, 280>([&](auto I) {
                constexpr int i = I, ks = (i - 160) / 6, n = (i - 160) % 6;
                if constexpr (i % 20 == 20 - L && i / 20 + 1 < 14) {
                    constexpr int wv = SP3.wait(i, K_W, V_P3 + i / 20 + 1, 4);
                    if constexpr (i / 20 + 1 == 10) { wait_vm<cmin(wv, SP3.wait(i, K_X, X_HST, 3))>(); bar(); if (tid == 0) signal(ws, C_HA + l * 16 + rp); }
                    else { wait_vm<wv>(); bar(); }
                }
                if constexpr (i == 274) sfor<0, 6>([&](auto N) { cs[N] = *(LAS f32x4*)(lds + L_CSG + ((8 + N) * 16 + 4 * g) * 4); });
                mma<ks == 0>(accB[0][n], B[i % R], A[0][ks]);
                rd(IC<i + L>{}, IC<280>{});
                mma<ks == 0>(accB[1][n], B[i % R], A[1][ks]);
                sfor<SP3.before(i), SP3.before(i + 1)>(emit);
                if constexpr (i + 1 < 280) wait_lgkm<cmin(L - 1, 278 - i)>();
            });
            mma_settle();
            sfor<0, 6>([&](auto N) { mma_fence(accB[0][N]); mma_fence(accB[1][N]); });
            STAMP(l * 24 + 14);
            wait_vm<SP3.n - SP3.find(K_X, X_FLAG, 0) - 1>();
            pin(flagv);
            u32x2 t6[2];
            sfor<0, 2>([&](auto M) {
                constexpr int m = M;
                const u32x2 lo = h_tile(accB[m][0], accB[m][1], cs[0], cs[1], m), hi = h_tile(accB[m][2], accB[m][3], cs[2], cs[3], m);
                st_v(hb + (size_t)(m * KSF + h_pos_b(cg)) * FRAG, voff, (u32x4){lo[0], lo[1], hi[0], hi[1]});
                t6[m] = h_tile(accB[m][4], accB[m][5], cs[4], cs[5], m);
            });
            {
                const auto s0 = __builtin_amdgcn_permlane16_swap(t6[0][0], t6[1][0], false, false);
                const auto s1 = __builtin_amdgcn_permlane16_swap(t6[0][1], t6[1][1], false, false);
                const u32x4 w = {s0[0], s1[0], s0[1], s1[1]};
                const char* dst = cg < 12 ? hb + (size_t)h_pos_b(16 + (cg >> 1)) * FRAG : ws + WS_DUMMY;
                const unsigned off = cg < 12 ? (g & 1) * KSF * FRAG + (2 * (cg & 1) + (g >> 1)) * 256 + r * 16 : (blockIdx.x * 4 + wid) * FRAG + voff;
                st_v(dst, off, w);
            }
            if (__builtin_amdgcn_readfirstlane(flagv) < 16) spin_ge(ws, C_HA + l * 16 + (rt >> 1), 16u);
            STAMP(l * 24 + 15);
        }
        proj(IC<4>{});
        STAMP(l * 24 + 18);
    }
}

extern "C" void kernel_launch(void* const* d_in, const int* in_sizes, int n_in, void* d_out, int out_size, void* d_ws, size_t ws_size, hipStream_t stream) {
    char* ws = (char*)d_ws;
    (void)hipFuncSetAttribute((const void*)prep_kernel, hipFuncAttributeMaxDynamicSharedMemorySize, PREP_LDS);
    (void)hipFuncSetAttribute((const void*)main_kernel, hipFuncAttributeMaxDynamicSharedMemorySize, LDS_BYTES);
    PrepArgs pa{(const char*)d_in[0], (const char*)d_in[1], (const char*)d_in[2], (const char*)d_in[4], (const char*)d_in[5], (const char*)d_in[6], ws, (const char*)d_out};
    hipLaunchKernelGGL(prep_kernel, dim3(PREP_UNITS), dim3(256), PREP_LDS, stream, pa);
    MainArgs ma{ws, (char*)d_out};
    hipLaunchKernelGGL(main_kernel, dim3(256), dim3(256), LDS_BYTES, stream, ma);
}
```

```cpp
#define PRETOUCH (256 << 10)
#include <hip/hip_runtime.h>
#include <hip/hip_bf16.h>
#include <type_traits>

#define LAS __attribute__((address_space(3)))
#define GAS __attribute__((address_space(1)))
#define DI __device__ __forceinline__
typedef short bf16x8 __attribute__((ext_vector_type(8)));
typedef short s16x4 __attribute__((ext_vector_type(4)));
typedef float f32x4 __attribute__((ext_vector_type(4)));
typedef float f32x2 __attribute__((ext_vector_type(2)));
typedef unsigned u32x4 __attribute__((ext_vector_type(4)));
typedef unsigned u32x2 __attribute__((ext_vector_type(2)));
typedef __bf16 bf16x2_n __attribute__((ext_vector_type(2)));
typedef GAS unsigned gu32;

constexpr int SEQ = 2048, DM = 640;
constexpr int KSD = 20, KSF = 56, NMT = 128;
constexpr int FRAG = 1024;

constexpr size_t WS_CTL = 0;
constexpr int CTL_LINES = 256;
constexpr int C_TMO = 0, C_KV = 1, C_O = 33, C_X1 = 65, C_HA = 97, C_HB = 129, C_X2 = 161;
constexpr size_t CTL_BYTES = (size_t)CTL_LINES * 256;
constexpr size_t WS_WQKV = CTL_BYTES;
constexpr size_t WS_WO = WS_WQKV + (size_t)16 * 160 * FRAG;
constexpr size_t WS_WGU = WS_WO + (size_t)8 * 100 * FRAG;
constexpr size_t WS_WD = WS_WGU + (size_t)16 * 280 * FRAG;
constexpr size_t WS_CSQ = WS_WD + (size_t)8 * 280 * FRAG;
constexpr size_t WS_CSG = WS_CSQ + (size_t)16 * 128 * 20 * 4;
constexpr size_t XT_BYTES = (size_t)NMT * KSD * FRAG;
constexpr size_t WS_X = WS_CSG + (size_t)16 * 224 * 20 * 4;
constexpr size_t ST_BYTES = (size_t)SEQ * 64;
constexpr size_t WS_ST = WS_X + 4 * XT_BYTES;
constexpr size_t WS_O = WS_ST + 4 * ST_BYTES;
constexpr size_t HT_BYTES = (size_t)NMT * KSF * FRAG;
constexpr size_t WS_H = WS_O + 2 * XT_BYTES;
constexpr size_t KVP_SLOT = 3584;
constexpr size_t WS_KVP = WS_H + 2 * HT_BYTES;
constexpr size_t WS_DUMMY = WS_KVP + (size_t)2 * 16 * 16 * KVP_SLOT;
constexpr size_t WS_END = WS_DUMMY + (size_t)1024 * FRAG;

template <int I, int N, class F> DI void sfor(F&& f) { if constexpr (I < N) { f(std::integral_constant<int, I>{}); sfor<I + 1, N>(f); } }
template <int V> using IC = std::integral_constant<int, V>;

DI unsigned pk_bf16(float lo, float hi) { const bf16x2_n v = __builtin_convertvector((f32x2){lo, hi}, bf16x2_n); return __builtin_bit_cast(unsigned, v); }
DI float bf_lo(unsigned u) { return __uint_as_float(u << 16); }
DI float bf_hi(unsigned u) { return __uint_as_float(u & 0xffff0000u); }
DI float xg_sum(float v) {
    const unsigned u = __float_as_uint(v);
    const auto a = __builtin_amdgcn_permlane16_swap(u, u, false, false);
    const float s = __uint_as_float(a[0]) + __uint_as_float(a[1]);
    const unsigned w = __float_as_uint(s);
    const auto b = __builtin_amdgcn_permlane32_swap(w, w, false, false);
    return __uint_as_float(b[0]) + __uint_as_float(b[1]);
}

template <int N> DI void wait_vm() { static_assert(N >= 0 && N < 64, "vmcnt range"); asm volatile("s_waitcnt vmcnt(%0)" ::"i"(N) : "memory"); }
template <int N> DI void wait_lgkm() { static_assert(N >= 0 && N < 16, "lgkmcnt range"); asm volatile("s_waitcnt lgkmcnt(%0)" ::"i"(N) : "memory"); }
DI void bar() { asm volatile("s_barrier" ::: "memory"); }
template <int OFF> DI void ds_rd(bf16x8& d, unsigned addr) { asm volatile("ds_read_b128 %0, %1 offset:%2" : "=v"(d) : "v"(addr), "i"(OFF) : "memory"); }
template <int T> DI void ds_rd_at(bf16x8& d, unsigned lo, unsigned hi) { ds_rd<T % 65536>(d, T < 65536 ? lo : hi); }
template <int R, int OFF> DI void ld_a(const char* sb, unsigned voff) { asm volatile("global_load_dwordx4 a[%c0:%c1], %2, %3 offset:%4 sc1" ::"i"(R), "i"(R + 3), "v"(voff), "s"(sb), "i"(OFF) : "memory"); }
template <int R, int OFF> DI void ld_w(const char* sb, unsigned voff) { asm volatile("global_load_dwordx4 a[%c0:%c1], %2, %3 offset:%4" ::"i"(R), "i"(R + 3), "v"(voff), "s"(sb), "i"(OFF) : "memory"); }
template <int OFF> DI void ld_v(u32x4& d, const char* sb, unsigned voff) { asm volatile("global_load_dwordx4 %0, %1, %2 offset:%3 sc1" : "=v"(d) : "v"(voff), "s"(sb), "i"(OFF) : "memory"); }
DI void ld_v2(u32x2& d, const char* sb, unsigned voff) { asm volatile("global_load_dwordx2 %0, %1, %2 sc1" : "=v"(d) : "v"(voff), "s"(sb) : "memory"); }
template <int OFF = 0> DI void ld_v1(unsigned& d, const char* sb, unsigned voff) { asm volatile("global_load_dword %0, %1, %2 offset:%3 sc1" : "=v"(d) : "v"(voff), "s"(sb), "i"(OFF) : "memory"); }
DI void st_v(const char* sb, unsigned voff, const u32x4& v) { asm volatile("global_store_dwordx4 %0, %1, %2 sc1\n\ts_nop 1" ::"v"(voff), "v"(v), "s"(sb) : "memory"); }
DI void st_v2(const char* sb, unsigned voff, const u32x2& v) { asm volatile("global_store_dwordx2 %0, %1, %2 sc1\n\ts_nop 1" ::"v"(voff), "v"(v), "s"(sb) : "memory"); }
DI void set_m0(unsigned lds) { asm volatile("s_mov_b32 m0, %0\n\ts_nop 0" ::"s"(lds) : "memory"); }
template <int OFF> DI void dma(const char* sb, unsigned voff) { asm volatile("global_load_lds_dwordx4 %0, %1 offset:%2" ::"v"(voff), "s"(sb), "i"(OFF) : "memory"); }
template <int P> DI void dma_piece(const char* sb, unsigned lds, unsigned voff) {
    if constexpr (P % 4 == 0) { set_m0(lds + P * FRAG); }
    dma<(P % 4) * FRAG>(sb + (P / 4) * 4 * FRAG, voff);
}
template <bool FIRST, int R> DI void mma(f32x4& c, const bf16x8& w) {
    if constexpr (FIRST) asm volatile("v_mfma_f32_16x16x32_bf16 %0, %1, a[%c2:%c3], 0" : "=v"(c) : "v"(w), "i"(R), "i"(R + 3));
    else asm volatile("v_mfma_f32_16x16x32_bf16 %0, %1, a[%c2:%c3], %0" : "+v"(c) : "v"(w), "i"(R), "i"(R + 3));
}
template <bool FIRST, int R> DI void mma_t(f32x4& c, const bf16x8& w) {
    if constexpr (FIRST) asm volatile("v_mfma_f32_16x16x32_bf16 %0, a[%c2:%c3], %1, 0" : "=v"(c) : "v"(w), "i"(R), "i"(R + 3));
    else asm volatile("v_mfma_f32_16x16x32_bf16 %0, a[%c2:%c3], %1, %0" : "+v"(c) : "v"(w), "i"(R), "i"(R + 3));
}
DI void mma_settle() { asm volatile("s_nop 7\n\ts_nop 7" ::: "memory"); }
DI void mma_fence(f32x4& c) { asm volatile("" : "+v"(c)); }
template <class T> DI void pins(T& x) { asm volatile("" : "+s"(x)); }
template <class T> DI void pin(T& x) { asm volatile("" : "+v"(x)); }

DI void kv_mfmas(f32x4 (&kv)[8], const u32x4 (&op)[5], const u32x4& v2) {
    asm volatile("s_nop 1\n\t"
                 "v_mfma_f32_16x16x32_bf16 %0, %8, %11, 0\n\tv_mfma_f32_16x16x32_bf16 %1, %8, %12, 0\n\tv_mfma_f32_16x16x32_bf16 %2, %8, %13, 0\n\t"
                 "v_mfma_f32_16x16x32_bf16 %3, %9, %11, 0\n\tv_mfma_f32_16x16x32_bf16 %4, %9, %12, 0\n\tv_mfma_f32_16x16x32_bf16 %5, %9, %13, 0\n\t"
                 "v_mfma_f32_16x16x32_bf16 %6, %10, %12, 0\n\tv_mfma_f32_16x16x32_bf16 %7, %10, %13, 0\n\ts_nop 7"
                 : "=&v"(kv[0]), "=&v"(kv[1]), "=&v"(kv[2]), "=&v"(kv[3]), "=&v"(kv[4]), "=&v"(kv[5]), "=&v"(kv[6]), "=&v"(kv[7])
                 : "v"(op[0]), "v"(op[1]), "v"(op[2]), "v"(op[3]), "v"(op[4]), "v"(v2));
}
DI void attn_mfmas(f32x4& t0, f32x4& t1, f32x4& t2, f32x4& dA, f32x4& dB, const u32x4 (&im)[5], const u32x4& da, const u32x4& db, const u32x4& qa, const u32x4& qb) {
    asm volatile("s_nop 1\n\t"
                 "v_mfma_f32_16x16x32_bf16 %3, %10, %12, 0\n\tv_mfma_f32_16x16x32_bf16 %4, %11, %13, 0\n\t"
                 "v_mfma_f32_16x16x32_bf16 %0, %5, %12, 0\n\tv_mfma_f32_16x16x32_bf16 %1, %7, %12, 0\n\tv_mfma_f32_16x16x32_bf16 %2, %9, %13, 0\n\t"
                 "v_mfma_f32_16x16x32_bf16 %0, %6, %13, %0\n\tv_mfma_f32_16x16x32_bf16 %1, %8, %13, %1\n\ts_nop 7"
                 : "=&v"(t0), "=&v"(t1), "=&v"(t2), "=&v"(dA), "=&v"(dB)
                 : "v"(im[0]), "v"(im[1]), "v"(im[2]), "v"(im[3]), "v"(im[4]), "v"(da), "v"(db), "v"(qa), "v"(qb));
}
DI float phi(float t) { return (t > 0.f ? t + 1.f : __expf(t)) + 1e-6f; }

__host__ __device__ constexpr int h_tile_base(int f) { return f <= 12 ? 7 * f : 84 + 6 * (f - 12); }
__host__ __device__ constexpr int h_pos_a(int f, int p) { return ((2 * f + p) >> 3) * 14 + ((2 * f + p) & 7); }
__host__ __device__ constexpr int h_pos_b(int ib) { return ib < 12 ? (ib / 6) * 14 + 8 + ib % 6 : (2 + (ib - 12) / 5) * 14 + 8 + (ib - 12) % 5; }

#define STAMP(i)

DI void map16x16(int b, int& r, int& c) { const int x = b & 7, q = b >> 3; r = (x >> 2) * 8 + (q & 7); c = (x & 3) * 4 + (q >> 3); }
DI void map32x8(int b, int& r, int& c) { const int x = b & 7, q = b >> 3; r = (x >> 1) * 8 + (q & 7); c = (x & 1) * 4 + (q >> 3); }

constexpr int PU_X = 32, PU_Q = 40, PU_G = 80, PU_O = 20, PU_D = 54, PREP_UNITS = PU_X + PU_Q + PU_G + PU_O + PU_D;
constexpr int PREP_LDS = 80 * 1024 + 256;
struct PrepArgs { const char *x, *wqkv, *wo, *wg, *wu, *wd; char* ws; const char* out; };

template <int N> DI void tr_frags(bf16x8 (&fr)[N], const unsigned (&addr)[N], unsigned rs) {
    s16x4 lo[N], hi[N];
#pragma unroll
    for (int i = 0; i < N; ++i) asm volatile("ds_read_b64_tr_b16 %0, %2\n\tds_read_b64_tr_b16 %1, %3" : "=&v"(lo[i]), "=&v"(hi[i]) : "v"(addr[i]), "v"(addr[i] + 4 * rs) : "memory");
    asm volatile("s_waitcnt lgkmcnt(0)" ::: "memory");
#pragma unroll
    for (int i = 0; i < N; ++i) { asm volatile("" : "+v"(lo[i]), "+v"(hi[i])); fr[i] = __builtin_shufflevector(lo[i], hi[i], 0, 1, 2, 3, 4, 5, 6, 7); }
}
DI void prep_st16(char* dst, const u32x4& v) { asm volatile("global_store_dwordx4 %0, %1, off sc1\n\ts_nop 1" ::"v"(dst), "v"(v) : "memory"); }
DI void prep_store(char* dst, const bf16x8& fr, int lane) { prep_st16(dst + lane * 16, __builtin_bit_cast(u32x4, fr)); }
DI void prep_colsum(float* dst, const bf16x8& fr, int lane) {
    const bf16x8 ones = {0x3f80, 0x3f80, 0x3f80, 0x3f80, 0x3f80, 0x3f80, 0x3f80, 0x3f80};
    const f32x4 z = {0.f, 0.f, 0.f, 0.f};
    const f32x4 d = __builtin_amdgcn_mfma_f32_16x16x32_bf16(fr, ones, z, 0, 0, 0);
    if ((lane & 15) == 0) prep_st16((char*)(dst + (lane >> 4) * 4), __builtin_bit_cast(u32x4, d));
}
DI int wswz(int row) { return 2 * ((row & 3) + 4 * ((row >> 3) & 1)); }
DI unsigned wimg_addr(unsigned img, unsigned rs, int row, unsigned off) { return img + row * rs + ((((off >> 4) ^ wswz(row)) << 4) | (off & 15)); }
DI void prep_dma(const char* src, LAS char* dst) { __builtin_amdgcn_global_load_lds((const GAS unsigned*)src, (LAS unsigned*)dst, 16, 0, 0); }

__global__ __launch_bounds__(256, 2) void prep_kernel(PrepArgs a) {
    extern __shared__ __attribute__((aligned(16))) char shm[];
    const int tid = threadIdx.x, wid = __builtin_amdgcn_readfirstlane(tid >> 6), lane = tid & 63;
    int u = blockIdx.x;
    const int g = lane >> 4, q = (lane & 15) >> 2, p = lane & 3, c16 = lane & 15;
    LAS char* img = (LAS char*)shm;
    const unsigned imgaddr = (unsigned)(size_t)shm;
    const bf16x8 zero = {0, 0, 0, 0, 0, 0, 0, 0};
#define PSTAMP(x)
#define POUT()
    if (wid == 0) {
        constexpr int NP = (int)((WS_END + PRETOUCH - 1) / PRETOUCH), NO = (SEQ * DM * 2 + PRETOUCH - 1) / PRETOUCH, PER = (NP + NO + 27) / 28;
        const int q = blockIdx.x >> 3;
#pragma unroll
        for (int it = 0; it < (PER + 63) / 64; ++it) {
            const int k = it * 64 + lane, pg = q * PER + k;
            if (k < PER && pg < NP + NO) { const char* p = pg < NP ? a.ws + (size_t)pg * PRETOUCH : a.out + (size_t)(pg - NP) * PRETOUCH;
                __builtin_amdgcn_global_load_lds((const GAS unsigned*)p, (LAS unsigned*)(img + 80 * 1024), 4, 0, 0); }
        }
    }
    if (u < PU_Q) {
        const int ks = u >> 1, hh = u & 1;
#pragma unroll
        for (int it = 0; it < 16; ++it) {
            const int i = (it * 4 + wid) * 64 + lane, row = i / 128, cs = i % 128, cc = cs ^ wswz(row), seg = cc / 40, c = cc % 40;
            prep_dma(a.wqkv + (size_t)(ks * 32 + row) * 3840 + (cc < 120 ? seg * 1280 + hh * 640 + c * 16 : hh * 640), img + (it * 4 + wid) * 1024);
        }
        wait_vm<0>(); __syncthreads(); PSTAMP(pt1);
        const unsigned rs = 2048;
#pragma unroll
        for (int e = 0; e < 2; ++e) {
            const int hl = wid * 2 + e, hp = hh * 8 + hl, row = 8 * g + q;
            const unsigned qo = hl * 80, ko = qo + 640, vo = qo + 1280;
            const unsigned of[8] = {ko + 8 * p, ko + 32 + 8 * p, p < 2 ? ko + 64 + 8 * p : vo + 64 + 8 * (p - 2), vo + 8 * p, vo + 32 + 8 * p, qo + 8 * p, qo + 40 + 8 * p, qo + (p == 0 ? 32 : 72)};
            unsigned ad[8];
#pragma unroll
            for (int nt = 0; nt < 8; ++nt) ad[nt] = wimg_addr(imgaddr, rs, row, of[nt]);
            bf16x8 fr[8];
            tr_frags<8>(fr, ad, rs);
            if (c16 >= 8) fr[7] = zero;
            char* w1 = a.ws + WS_WQKV + (size_t)hp * 160 * FRAG + (size_t)ks * 5 * FRAG;
            char* w2 = a.ws + WS_WQKV + (size_t)hp * 160 * FRAG + (size_t)(100 + ks * 3) * FRAG;
            float* cs = (float*)(a.ws + WS_CSQ) + (hp * 20 + ks) * 128;
#pragma unroll
            for (int nt = 0; nt < 8; ++nt) { prep_store(nt < 5 ? w1 + nt * FRAG : w2 + (nt - 5) * FRAG, fr[nt], lane); prep_colsum(cs + nt * 16, fr[nt], lane); }
        }
        PSTAMP(pt2); POUT();
        return;
    }
    u -= PU_Q;
    if (u < PU_X) {
        if (u == 0) __hip_atomic_store((gu32*)(a.ws + WS_CTL + tid * 256), 0u, __ATOMIC_RELAXED, __HIP_MEMORY_SCOPE_AGENT);
        const int mt = u * 4 + wid, r = lane & 15;
        const char* src = a.x + (size_t)mt * 16 * 1280;
        char* dst = a.ws + WS_X + (size_t)mt * KSD * FRAG + lane * 16;
        LAS char* rows = img + wid * 20 * 1024;
#pragma unroll
        for (int it = 0; it < 20; ++it) { const int i = it * 64 + lane, row = i / 80, c = (i % 80) ^ row; prep_dma(src + row * 1280 + c * 16, rows + it * 1024); }
        wait_vm<0>();
        u32x4 v[20];
#pragma unroll
        for (int ks = 0; ks < 20; ++ks) v[ks] = *(LAS u32x4*)(rows + r * 1280 + ((ks * 4 + g) ^ r) * 16);
        const bf16x8 ones = {0x3f80, 0x3f80, 0x3f80, 0x3f80, 0x3f80, 0x3f80, 0x3f80, 0x3f80};
        f32x4 s1 = {0.f, 0.f, 0.f, 0.f}, s2 = {0.f, 0.f, 0.f, 0.f};
#pragma unroll
        for (int ks = 0; ks < 20; ++ks) {
            prep_st16(dst + ks * FRAG, v[ks]);
            const bf16x8 xf = __builtin_bit_cast(bf16x8, v[ks]);
            s1 = __builtin_amdgcn_mfma_f32_16x16x32_bf16(xf, ones, s1, 0, 0, 0);
            s2 = __builtin_amdgcn_mfma_f32_16x16x32_bf16(xf, xf, s2, 0, 0, 0);
        }
        const int j = r & 3;
        const float a1 = j == 0 ? s1[0] : j == 1 ? s1[1] : j == 2 ? s1[2] : s1[3], a2 = j == 0 ? s2[0] : j == 1 ? s2[1] : j == 2 ? s2[2] : s2[3];
        const bool own = (r >> 2) == g;
        const int chunk = (g - (r >> 2)) & 3;
        prep_st16(a.ws + WS_ST + (size_t)(mt * 16 + r) * 64 + chunk * 16, own ? (u32x4){__float_as_uint(a1), __float_as_uint(a2), 0u, 0u} : (u32x4){0u, 0u, 0u, 0u});
        PSTAMP(pt1); PSTAMP(pt2); POUT();
        return;
    }
    u -= PU_X;
    if (u < PU_G) {
        const int ks = u >> 2, fq = u & 3;
#pragma unroll
        for (int it = 0; it < 14; ++it) {
            const int i = (it * 4 + wid) * 64 + lane, row = i / 112, cc = (i % 112) ^ wswz(row), up = cc / 56; int c = cc % 56;
            if (fq == 3 && c >= 48) c = 0;
            prep_dma((up ? a.wu : a.wg) + (size_t)(ks * 32 + row) * 3456 + fq * 896 + c * 16, img + (it * 4 + wid) * 1024);
        }
        wait_vm<0>(); __syncthreads(); PSTAMP(pt1);
        const int f = fq * 4 + wid, nt7 = f < 12;
        const unsigned rs = 1792, colo = (h_tile_base(f) - h_tile_base(fq * 4)) * 32;
        unsigned ad[14];
#pragma unroll
        for (int nt = 0; nt < 14; ++nt) { const int j = nt >> 1, up = nt & 1;
            ad[nt] = wimg_addr(imgaddr, rs, 8 * g + q, colo + up * 896 + 2 * (j < 6 ? 32 * (j >> 1) + 8 * p + 4 * (j & 1) : (nt7 ? 96 : 0) + 4 * p)); }
        bf16x8 fr[14];
        tr_frags<14>(fr, ad, rs);
        if (!nt7) { fr[12] = zero; fr[13] = zero; }
        char* da = a.ws + WS_WGU + (size_t)f * 280 * FRAG + (size_t)ks * 8 * FRAG;
        char* db = a.ws + WS_WGU + (size_t)f * 280 * FRAG + (size_t)(160 + ks * 6) * FRAG;
        float* cs = (float*)(a.ws + WS_CSG) + (f * 20 + ks) * 224;
#pragma unroll
        for (int nt = 0; nt < 14; ++nt) { prep_store(nt < 8 ? da + nt * FRAG : db + (nt - 8) * FRAG, fr[nt], lane); prep_colsum(cs + nt * 16, fr[nt], lane); }
        PSTAMP(pt2); POUT();
        return;
    }
    u -= PU_G;
    {
        const bool down = u >= PU_O;
        const int v = down ? u - PU_O : u;
        int kq, s, row_lo, row_hi;
        if (!down) { kq = v / 5; s = v % 5; row_lo = v * 32; row_hi = row_lo + 16; }
        else if (v < 32) { kq = v >> 3; s = v & 7; row_lo = 16 * (h_tile_base(v >> 1) + 2 * (v & 1)); row_hi = row_lo + 16; }
        else {
            const int ib = v - 32, pos = h_pos_b(ib); kq = pos / 14; s = pos % 14;
            if (ib < 16) { row_lo = 16 * (h_tile_base(ib) + 4); row_hi = row_lo + 16; }
            else { const int f0 = 2 * (ib - 16); row_lo = 16 * (h_tile_base(f0) + 6); row_hi = 16 * (h_tile_base(f0 + 1) + 6); }
        }
        const char* w = down ? a.wd : a.wo;
#pragma unroll
        for (int it = 0; it < 10; ++it) {
            const int i = (it * 4 + wid) * 64 + lane, row = i / 80, c = (i % 80) ^ wswz(row);
            const int wrow = row < 16 ? row_lo + row : row_hi + row - 16;
            prep_dma(w + (size_t)wrow * 1280 + c * 16, img + (it * 4 + wid) * 1024);
        }
        wait_vm<0>(); __syncthreads(); PSTAMP(pt1);
        const unsigned rs = 1280;
#pragma unroll
        for (int e = 0; e < 2; ++e) {
            const int c8 = wid * 2 + e;
            const int pa = (c8 & 1) ? 16 : 0, hf = (c8 & 1) ? 0 : 64;
            unsigned ad[5];
#pragma unroll
            for (int nt = 0; nt < 5; ++nt) ad[nt] = wimg_addr(imgaddr, rs, 8 * g + q, c8 * 160 + 2 * (nt < 4 ? pa + 32 * (nt >> 1) + 8 * p + 4 * (nt & 1) : hf + 4 * p));
            bf16x8 fr[5];
            tr_frags<5>(fr, ad, rs);
            char* dst = a.ws + (down ? WS_WD + (size_t)((c8 * 4 + kq) * 14 + s) * 5 * FRAG : WS_WO + (size_t)((c8 * 4 + kq) * 5 + s) * 5 * FRAG);
#pragma unroll
            for (int nt = 0; nt < 5; ++nt) prep_store(dst + nt * FRAG, fr[nt], lane);
            if (down && kq >= 2 && s == 12) {
#pragma unroll
                for (int nt = 0; nt < 5; ++nt) prep_store(dst + (5 + nt) * FRAG, zero, lane);
            }
        }
        PSTAMP(pt2); POUT();
    }
}

enum { K_A = 0, K_W = 1, K_X = 2 };
struct Op { int pos, kind, a, b; };
template <int N> struct Sched {
    Op op[N] = {}; int n = 0;
    constexpr void add(int pos, int kind, int a, int b) { op[n] = Op{pos, kind, a, b}; ++n; }
    constexpr void sort() { for (int i = 1; i < n; ++i) { const Op t = op[i]; int j = i - 1; while (j >= 0 && op[j].pos > t.pos) { op[j + 1] = op[j]; --j; } op[j + 1] = t; } }
    constexpr int before(int pos) const { int c = 0; for (int i = 0; i < n; ++i) c += op[i].pos < pos; return c; }
    constexpr int find(int kind, int a, int b) const { for (int i = 0; i < n; ++i) if (op[i].kind == kind && op[i].a == a && op[i].b == b) return i; return -1; }
    constexpr int wait(int pos, int kind, int a, int b) const { const int i = find(kind, a, b); if (i < 0) return 63; const int w = before(pos) - i - 1; return w < 0 ? -1 : (w > 63 ? 63 : w); }
};
constexpr int cmin(int a, int b) { return a < b ? a : b; }

#define K_HASIG 184
#define K_HAFLAG 236
#define K_HACHK 254
constexpr int NSLOT = 6, SLOT = 20 * FRAG;
constexpr int V_P1 = 0, V_P3 = 12, V_END = 30;
constexpr int slot_of(int v) { return (v % NSLOT) * SLOT; }

constexpr int xa_reg(int m, int ks) { return (m * 20 + ks) * 4; }
constexpr int w2_reg(int j) { return j < 24 ? 160 + 4 * j : xa_reg(0, 0); }
constexpr int w4_reg(int j) { return j < 24 ? 160 + 4 * j : (j < 64 ? xa_reg((j - 24) & 1, (j - 24) >> 1) : 160 + 4 * (j - 64)); }

enum { X_STAT = 0, X_KVST = 1, X_HST = 2, X_FLAG = 3, X_CS = 4, X_RES = 5, X_WP = 6, X_HA = 7 };

template <bool FIRST> constexpr Sched<160> make_p1() {
    Sched<160> s;
    if (FIRST) {
        for (int p = 0; p < 5; ++p) s.add(-1, K_W, 0, p);
        for (int k = 0; k < 2; ++k) { s.add(-1, K_A, k, 0); s.add(-1, K_A, k, 1); }
        for (int p = 0; p < 5; ++p) s.add(-1, K_W, 1, p);
        s.add(-1, K_X, X_STAT, 0); s.add(-1, K_X, X_STAT, 1);
        int pos = 0;
        for (int k = 2; k < 20; ++k) {
            s.add(pos++, K_A, k, 0); s.add(pos++, K_A, k, 1);
            if (k == 3 || k == 7 || k == 11) for (int p = 0; p < 5; ++p) s.add(pos++, K_W, 2 + (k - 3) / 4, p);
        }
        for (int p = 0; p < 5; ++p) s.add(pos++, K_W, 5, p);
        pos = 61;
        for (int c = 0; c < 2; ++c) for (int p = 0; p < 5; ++p) { s.add(pos, K_W, 6 + c, p); pos += 3; }
        for (int i = 0; i < 40; ++i) s.add(57 + i, K_X, X_CS, i);
        for (int i = 0; i < 3; ++i) s.add(92 + 2 * i, K_X, X_RES, i);
    } else {
        for (int k = 0; k < 5; ++k) { s.add(-1, K_A, k, 0); s.add(-1, K_A, k, 1); }
        s.add(-1, K_X, X_STAT, 0); s.add(-1, K_X, X_STAT, 1);
        for (int k = 5; k < 20; ++k) { s.add(5 * (k - 5), K_A, k, 0); s.add(5 * (k - 5) + 2, K_A, k, 1); }
        const int w6[5] = {64, 69, 74, 78, 82}, w7[5] = {86, 89, 92, 95, 98};
        for (int p = 0; p < 5; ++p) { s.add(w6[p], K_W, 6, p); s.add(w7[p], K_W, 7, p); }
    }
    s.sort(); return s;
}
constexpr Sched<128> make_p1b() {
    Sched<128> s;
    s.add(-1, K_X, X_KVST, 0);
    for (int j = 0; j < 25; ++j) s.add(6 + 2 * j, K_X, X_WP, j);
    s.sort(); return s;
}
constexpr Sched<128> make_p2() {
    Sched<128> s;
    for (int k = 0; k < 3; ++k) for (int m = 0; m < 4; ++m) s.add(-1, K_A, k, m);
    for (int k = 3; k < 5; ++k) for (int m = 0; m < 4; ++m) s.add(((k - 3) * 4 + m) * 4 + 1, K_A, k, m);
    s.sort(); return s;
}
constexpr int W4_G = 12, W4_A = 24, W4_B = 54;
constexpr int P4_AW = 5;
constexpr Sched<192> make_p3() {
    Sched<192> s;
    for (int k = 0; k < 4; ++k) { s.add(-1, K_A, k, 0); s.add(-1, K_A, k, 1); }
    s.add(-1, K_X, X_STAT, 0); s.add(-1, K_X, X_STAT, 1);
    for (int k = 4; k < 20; ++k) { s.add(8 * (k - 4) + 1, K_A, k, 0); s.add(8 * (k - 4) + 5, K_A, k, 1); }
    for (int c = 2; c < 10; ++c) for (int p = 0; p < 5; ++p) { int pos = 20 * c - 6 + 1 + p * 4; if (c == 8 && pos < 160) pos = 160; s.add(pos, K_W, V_P3 + c + 4, p); }
    for (int j = W4_G; j < W4_A; ++j) s.add(111 + 4 * (j - W4_G), K_X, X_WP, j);
    for (int i = 0; i < 4; ++i) s.add(159, K_X, X_HST, i);
    for (int j = W4_A; j < W4_B; ++j) s.add(166 + 3 * (j - W4_A), K_X, X_WP, j);
    s.add(K_HAFLAG, K_X, X_FLAG, 0);
    for (int j = 0; j < 16; ++j) s.add(K_HACHK + 1 + (j * (279 - K_HACHK - 1)) / 16, K_X, X_HA, j);
    s.sort(); return s;
}
constexpr Sched<160> make_p4() {
    Sched<160> s;
    for (int i = 0; i < 3; ++i) s.add(-1, K_X, X_HST, i);
    for (int k = 4; k < 8; ++k) for (int m = 0; m < 4; ++m) s.add(20 * (k - 4) + 1 + 5 * m, K_A, k, m);
    for (int j = W4_B; j < 70; ++j) { const int t = j - W4_B; s.add(t < 4 ? 20 * t + 19 : 82 + (t - 4) * 5, K_X, X_WP, j); }
    s.add(112, K_X, X_FLAG, 0);
    for (int k = 8; k < 14; ++k) for (int m = 0; m < 4; ++m) { const int t = (k - 8) * 4 + m; s.add(t < 8 ? 140 + 2 * t : 160 + 20 * (k - 10) + 4 * m, K_A, k, m); }
    s.sort(); return s;
}
template <bool FIRST> constexpr Sched<160> SP1 = make_p1<FIRST>();
constexpr auto SP1B = make_p1b();
constexpr auto SP3 = make_p3();
constexpr auto SP2 = make_p2();
constexpr auto SP4 = make_p4();

constexpr int L_RING = 0, L_SCR = NSLOT * SLOT, L_IMG = L_SCR + 8 * FRAG, L_XCH = 4 * SLOT, L_TAB = L_SCR + 32 * FRAG, L_CSQ = L_TAB + 1024, L_CSG = L_CSQ + 512, L_STAMP = L_CSG + 1024,
              LDS_BYTES = L_STAMP + 512;
struct MainArgs { char* ws; char* out; };

DI void row_stats(const u32x4& p, float& rstd, float& mrs) {
    float s = __uint_as_float(p[0]) + __uint_as_float(p[2]), s2 = __uint_as_float(p[1]) + __uint_as_float(p[3]);
    s = xg_sum(s); s2 = xg_sum(s2);
    const float mean = s * (1.f / DM), var = fmaxf(s2 * (1.f / DM) - mean * mean, 0.f);
    rstd = __builtin_amdgcn_rsqf(var + 1e-5f);
    mrs = mean * rstd;
}
DI void spin_ge(const char* ws, int line, unsigned target) {
    gu32* p = (gu32*)(ws + WS_CTL + (size_t)line * 256);
    gu32* tmo = (gu32*)(ws + WS_CTL + C_TMO * 256);
    unsigned spins = 0;
    while ((unsigned)__builtin_amdgcn_readfirstlane(__hip_atomic_load(p, __ATOMIC_RELAXED, __HIP_MEMORY_SCOPE_AGENT)) < target) {
        __builtin_amdgcn_s_sleep(1);
        ++spins;
        if ((spins & 1023u) == 0u && __builtin_amdgcn_readfirstlane(__hip_atomic_load(tmo, __ATOMIC_RELAXED, __HIP_MEMORY_SCOPE_AGENT))) break;
        if (spins > (1u << 17)) { __hip_atomic_store(tmo, 1u, __ATOMIC_RELAXED, __HIP_MEMORY_SCOPE_AGENT); break; }
    }
    asm volatile("" ::: "memory");
}
DI void signal(char* ws, int line) { __hip_atomic_fetch_add((gu32*)(ws + WS_CTL + (size_t)line * 256), 1u, __ATOMIC_RELAXED, __HIP_MEMORY_SCOPE_AGENT); }
template <bool FIRST, int R> DI void mma_w(f32x4& c, const bf16x8& x) {
    if constexpr (FIRST) asm volatile("v_mfma_f32_16x16x32_bf16 %0, a[%c2:%c3], %1, 0" : "=v"(c) : "v"(x), "i"(R), "i"(R + 3));
    else asm volatile("v_mfma_f32_16x16x32_bf16 %0, a[%c2:%c3], %1, %0" : "+v"(c) : "v"(x), "i"(R), "i"(R + 3));
}
template <int OFF> DI void ld_x(bf16x8& d, const char* sb, unsigned voff) { asm volatile("global_load_dwordx4 %0, %1, %2 offset:%3 sc1" : "=v"(d) : "v"(voff), "s"(sb), "i"(OFF) : "memory"); }
template <int OFF> DI void ds_wr(unsigned addr, const f32x4& v) { asm volatile("ds_write_b128 %0, %1 offset:%2" ::"v"(addr), "v"(v), "i"(OFF) : "memory"); }

__global__ __launch_bounds__(256, 1) void main_kernel(MainArgs a) {
    extern __shared__ __attribute__((aligned(16))) char shm[];
    const unsigned lds0 = (unsigned)(size_t)shm;
    LAS char* lds = (LAS char*)shm;
    char* const ws = a.ws;
    int tid = threadIdx.x;
    const int wid = __builtin_amdgcn_readfirstlane(tid >> 6);
    int g = (tid & 63) >> 4, r = tid & 15;
    unsigned voff = (tid & 63) * 16;
    int rp, cg; map16x16(blockIdx.x, rp, cg);
    int rt, c8; map32x8(blockIdx.x, rt, c8);
    const int mt_a = rp * 8 + wid * 2;
    const int mtf = rt * 4 + wid, odd = c8 & 1;
    const int tA = odd ? (5 * c8 + 1) / 2 : 5 * c8 / 2, tH = odd ? (5 * c8 - 1) / 2 : 5 * c8 / 2 + 2;
    auto hoff_f = [&]() { return (unsigned)(tH * FRAG + (odd * 2 + (g >> 1)) * 256 + r * 16 + (g & 1) * 8); };
    asm volatile("" ::: "a0", "a255");
    STAMP(0);

    const char* wP1 = ws + WS_WQKV + (size_t)(cg * 160 + wid * 5) * FRAG;
    const char* wP3 = ws + WS_WGU + (size_t)(cg * 280 + wid * 5) * FRAG;
    const char* wP2 = ws + WS_WO + (size_t)((c8 * 4 + wid) * 25) * FRAG;
    const char* wP4 = ws + WS_WD + (size_t)((c8 * 4 + wid) * 70) * FRAG;
    unsigned wdst = lds0 + L_RING + wid * 5 * FRAG;
    auto fresh_ptrs = [&]() { pins(wP1); pins(wP2); pins(wP3); pins(wP4); pins(wdst); };
    unsigned rdb, rdb_hi;
    auto fresh_lanes = [&]() { pin(tid); g = (tid & 63) >> 4; r = tid & 15; voff = (tid & 63) * 16; rdb = lds0 + L_RING + voff; rdb_hi = rdb + 65536; };
    auto w_op = [&](auto V, auto P) {
        constexpr int v0 = V, v = v0 % V_END, p = P;
        const char* src = v < V_P3 ? wP1 + (size_t)(v - V_P1) * SLOT : wP3 + (size_t)(v - V_P3) * SLOT;
        dma_piece<p>(src, wdst + slot_of(v), voff);
    };
    auto w_chunk = [&](auto V) { sfor<0, 5>([&](auto P) { w_op(V, P); }); };

    unsigned kvo, kv_red, kv_img;
    {
        const int t = tid < 210 ? tid : 209; const bool z = t >= 200; const int hd0 = t / 100, v = t % 100;
        const int dg = z ? t - 200 : hd0 * 5 + v / 20, e = z ? 0 : hd0 * 20 + v % 20;
        const int dt = dg >> 2, gg = dg & 3, et = z ? 2 : e >> 4, cc = z ? 0 : (e < 32 ? e & 15 : e - 24);
        kvo = t * 16;
        kv_red = L_SCR + (dt == 2 ? 5 + et : dt * 3 + et) * FRAG + (gg * 16 + cc) * 16;
        const int hd = dg >= 5, dl = dg - hd * 5, ig = dl < 4 ? dl : hd, hb = dl < 4 ? 0 : 8;
        const int T = e >= 32 ? 2 : (e >> 2) & 1, c = e >= 32 ? e - 32 : (e >> 3) * 4 + (e & 3);
        kv_img = z ? L_IMG + 5 * FRAG + hd * 64 + ig * 16 + hb : L_IMG + (T == 2 ? 4 : T * 2 + hd) * FRAG + (ig * 16 + c) * 16 + hb;
        pin(kvo); pin(kv_red); pin(kv_img);
    }
    u32x4 resA, resB; u32x2 resH;
    float csk[5]; f32x4 csq[3];
    bf16x8 AH[P4_AW][4];

    for (int l = 0; l < 2; ++l) {
        {
            const char* xsrc = ws + WS_X + (size_t)(2 * l) * XT_BYTES + (size_t)mt_a * KSD * FRAG;
            const char* stsrc = ws + WS_ST + (size_t)(2 * l) * ST_BYTES + (size_t)mt_a * 16 * 64;
            char* kvp = ws + WS_KVP + (size_t)((l * 16 + cg) * 16) * KVP_SLOT;
            if (l > 0) { if (wid == 0) spin_ge(ws, C_X2 + rp, 16u); wait_vm<0>(); bar(); }
            STAMP(l * 24 + 1);
            fresh_lanes(); fresh_ptrs(); pins(xsrc);
            f32x4 acc1[2][5], acc2[2][3];
            float rstd[2], mrs[2];
            f32x4 st[2][2];
            auto pass1 = [&](auto FST) {
                constexpr bool first = FST;
                u32x4 stp[2]; unsigned cq[20], cgp[20];
                const char* csqb = ws + WS_CSQ + (size_t)cg * 20 * 512; const char* csgb = ws + WS_CSG + (size_t)cg * 20 * 896; const char* xr = ws + WS_X + (size_t)mtf * KSD * FRAG;
                pins(csqb); pins(csgb); pins(xr);
                auto emit = [&](auto K) {
                    constexpr Op o = SP1<first>.op[K];
                    if constexpr (o.kind == K_A) ld_a<xa_reg(o.b, o.a), (o.a % 4) * FRAG>(xsrc + (size_t)(o.b * KSD + (o.a / 4) * 4) * FRAG, voff);
                    else if constexpr (o.kind == K_W) w_op(IC<o.a>{}, IC<o.b>{});
                    else if constexpr (o.a == X_STAT) ld_v<o.b * FRAG>(stp[o.b], stsrc, r * 64 + g * 16);
                    else if constexpr (o.a == X_CS) {
                        if constexpr (o.b < 20) ld_v1<(o.b % 8) * 512>(cq[o.b], csqb + (o.b / 8) * 4096, (tid & 127) * 4);
                        else ld_v1<((o.b - 20) % 4) * 896>(cgp[o.b - 20], csgb + ((o.b - 20) / 4) * 3584, (tid < 224 ? tid : 0) * 4);
                    } else if constexpr (o.a == X_RES) {
                        if constexpr (o.b == 0) ld_v<0>(resA, xr + tA * FRAG, voff); else if constexpr (o.b == 1) ld_v<FRAG>(resB, xr + tA * FRAG, voff); else ld_v2(resH, xr, hoff_f());
                    }
                };
                sfor<0, SP1<first>.before(0)>(emit);
                constexpr int L = 4, R = 5;
                bf16x8 B[R];
                auto rd = [&](auto I) { constexpr int i = I; if constexpr (i < 100) ds_rd_at<slot_of(V_P1 + i / 20) + (i % 20) * FRAG>(B[i % R], rdb, rdb_hi); };
                if constexpr (first) { wait_vm<SP1<first>.wait(0, K_W, 0, 4)>(); bar(); }
                sfor<0, L>(rd);
                wait_lgkm<L - 1>();
                sfor<0, 100>([&](auto I) {
                    constexpr int i = I, ks = i / 5, n = i % 5;
                    if constexpr (n == 0) wait_vm<SP1<first>.wait(i, K_A, ks, 1)>();
                    if constexpr (i == 30) {
                        wait_vm<SP1<first>.wait(i, K_X, X_STAT, 1)>();
                        pin(stp[0]); pin(stp[1]);
                        row_stats(stp[0], rstd[0], mrs[0]); row_stats(stp[1], rstd[1], mrs[1]);
                        if (g == 0) {
                            *(LAS f32x2*)(lds + L_TAB + (wid * 32 + r) * 8) = (f32x2){rstd[0], mrs[0]};
                            *(LAS f32x2*)(lds + L_TAB + (wid * 32 + 16 + r) * 8) = (f32x2){rstd[1], mrs[1]};
                        }
                    }
                    if constexpr (i == 50) {
                        sfor<0, 2>([&](auto M) { st[M][0] = *(LAS f32x4*)(lds + L_TAB + (wid * 32 + M * 16 + 4 * g) * 8); st[M][1] = *(LAS f32x4*)(lds + L_TAB + (wid * 32 + M * 16 + 4 * g) * 8 + 16); });
                    }
                    if constexpr (i == 70) { pin(st[0][0]); pin(st[0][1]); pin(st[1][0]); pin(st[1][1]); pin(rstd[0]); pin(rstd[1]); pin(mrs[0]); pin(mrs[1]); }
                    if constexpr (i % 20 == 20 - L && i < 80 && first) { wait_vm<SP1<first>.wait(i, K_W, i / 20 + 1, 4)>(); bar(); }
                    if constexpr (i == 60) bar();
                    mma_t<ks == 0, xa_reg(0, ks)>(acc1[0][n], B[i % R]);
                    rd(IC<i + L>{});
                    mma_t<ks == 0, xa_reg(1, ks)>(acc1[1][n], B[i % R]);
                    sfor<SP1<first>.before(i), SP1<first>.before(i + 1)>(emit);
                    if constexpr (i + 1 < 100) wait_lgkm<cmin(L - 1, 98 - i)>();
                });
                if constexpr (first) {
                    wait_vm<cmin(SP1<first>.n - SP1<first>.find(K_X, X_RES, 2) - 1, SP1<first>.n - SP1<first>.find(K_X, X_CS, 39) - 1)>();
                    float sq = 0.f, sg = 0.f;
                    sfor<0, 20>([&](auto J) { constexpr int j = J; pin(cq[j]); pin(cgp[j]); sq += __uint_as_float(cq[j]); sg += __uint_as_float(cgp[j]); });
                    pin(resA); pin(resB); pin(resH);
                    if (tid < 128) *(LAS float*)(lds + L_CSQ + tid * 4) = sq;
                    if (tid < 224) *(LAS float*)(lds + L_CSG + tid * 4) = sg;
                    wait_lgkm<0>(); bar();
                    sfor<0, 5>([&](auto N) { csk[N] = *(LAS float*)(lds + L_CSQ + (N * 16 + r) * 4); });
                    sfor<0, 3>([&](auto N) { csq[N] = *(LAS f32x4*)(lds + L_CSQ + ((5 + N) * 16 + 4 * g) * 4); });
                }
            };
            if (l == 0) pass1(std::true_type{}); else pass1(std::false_type{});
            mma_settle();
            sfor<0, 5>([&](auto N) { mma_fence(acc1[0][N]); mma_fence(acc1[1][N]); });
            STAMP(l * 24 + 2);
            fresh_lanes();
            {
                u32x4 op[5];
                sfor<0, 5>([&](auto N) {
                    constexpr int n = N;
                    float v[2][4];
                    sfor<0, 2>([&](auto M) {
                        sfor<0, 4>([&](auto J) { constexpr int m = M, j = J;
                            float t = acc1[m][n][j] * st[m][j >> 1][(j & 1) * 2] - st[m][j >> 1][(j & 1) * 2 + 1] * csk[n];
                            if constexpr (n < 2) t = phi(t); else if constexpr (n == 2) t = r < 8 ? phi(t) : t;
                            v[m][j] = t; });
                    });
                    op[n] = (u32x4){pk_bf16(v[0][0], v[0][1]), pk_bf16(v[0][2], v[0][3]), pk_bf16(v[1][0], v[1][1]), pk_bf16(v[1][2], v[1][3])};
                });
                u32x4 v2 = op[2];
                if (r < 8) v2 = (u32x4){0x3f803f80u, 0x3f803f80u, 0x3f803f80u, 0x3f803f80u};
                f32x4 kv[8];
                kv_mfmas(kv, op, v2);
                sfor<0, 8>([&](auto T) { *(LAS f32x4*)(lds + L_SCR + (wid * 8 + T) * FRAG + voff) = kv[T]; });
            }
            wait_lgkm<0>(); bar();
            {
                const f32x4 sum = (*(LAS f32x4*)(lds + kv_red) + *(LAS f32x4*)(lds + kv_red + 8 * FRAG)) + (*(LAS f32x4*)(lds + kv_red + 16 * FRAG) + *(LAS f32x4*)(lds + kv_red + 24 * FRAG));
                st_v(kvp + rp * KVP_SLOT, kvo, __builtin_bit_cast(u32x4, sum));
            }
            STAMP(l * 24 + 3);
            fresh_ptrs();
            {
                constexpr int R2 = 6, L2 = 4;
                bf16x8 B2[R2];
                auto rd = [&](auto I) { constexpr int i = I; if constexpr (i < 60) ds_rd_at<slot_of(V_P1 + 5 + i / 20) + (i % 20) * FRAG>(B2[i % R2], rdb, rdb_hi); };
                auto emit = [&](auto K) { constexpr Op o = SP1B.op[K]; if constexpr (o.a == X_WP) ld_w<w2_reg(o.b), (o.b % 4) * FRAG>(wP2 + (size_t)(o.b / 4) * 4 * FRAG, voff); };
                sfor<0, L2>(rd);
                wait_lgkm<L2 - 1>();
                sfor<0, 60>([&](auto I) {
                    constexpr int i = I, ks = i / 3, n = i % 3;
                    if constexpr (i == 20 - L2) {
                        wait_vm<SP1B.wait(i, K_X, X_KVST, 0)>(); bar();
                        if (tid == 0) signal(ws, C_KV + l * 16 + cg);
                        *(LAS u32x4*)(lds + L_IMG + tid * 16) = (u32x4){0u, 0u, 0u, 0u};
                        if (tid < 128) *(LAS u32x4*)(lds + L_IMG + 4096 + tid * 16) = (u32x4){0u, 0u, 0u, 0u};
                    }
                    mma<ks == 0, xa_reg(0, ks)>(acc2[0][n], B2[i % R2]);
                    rd(IC<i + L2>{});
                    mma<ks == 0, xa_reg(1, ks)>(acc2[1][n], B2[i % R2]);
                    sfor<SP1B.before(i), SP1B.before(i + 1)>(emit);
                    if constexpr (i + 1 < 60) wait_lgkm<cmin(L2 - 1, 58 - i)>();
                });
            }
            mma_settle();
            sfor<0, 3>([&](auto N) { mma_fence(acc2[0][N]); mma_fence(acc2[1][N]); });
            STAMP(l * 24 + 4);
            fresh_lanes();
            if (wid == 0) spin_ge(ws, C_KV + l * 16 + cg, 16u);
            wait_lgkm<0>(); bar();
            STAMP(l * 24 + 5);
            u32x4 pv[16];
            sfor<0, 16>([&](auto I) { ld_v<0>(pv[I], kvp + I * KVP_SLOT, kvo); });
            u32x4 qa[2], qb[2];
            sfor<0, 2>([&](auto M) {
                constexpr int m = M;
                unsigned pk[3][2];
                sfor<0, 3>([&](auto N) {
                    constexpr int n = N;
                    float v[4];
                    sfor<0, 4>([&](auto J) { constexpr int j = J; v[j] = phi(acc2[m][n][j] * rstd[m] - mrs[m] * csq[n][j]); });
                    pk[n][0] = pk_bf16(v[0], v[1]); pk[n][1] = pk_bf16(v[2], v[3]);
                });
                qa[m] = (u32x4){pk[0][0], pk[0][1], pk[2][0], pk[2][1]};
                qb[m] = (u32x4){pk[1][0], pk[1][1], pk[2][0], pk[2][1]};
            });
            pin(qa[0]); pin(qa[1]); pin(qb[0]); pin(qb[1]);
            {
                wait_vm<0>();
                sfor<0, 16>([&](auto I) { pin(pv[I]); });
                f32x4 s0 = __builtin_bit_cast(f32x4, pv[0]) + __builtin_bit_cast(f32x4, pv[1]), s1 = __builtin_bit_cast(f32x4, pv[2]) + __builtin_bit_cast(f32x4, pv[3]);
#pragma unroll
                for (int i = 4; i < 16; i += 2) { s0 += __builtin_bit_cast(f32x4, pv[i]); s1 += __builtin_bit_cast(f32x4, pv[i + 1]); }
                const f32x4 s = s0 + s1;
                const u32x2 w = {pk_bf16(s[0], s[1]), pk_bf16(s[2], s[3])};
                *(LAS u32x2*)(lds + kv_img) = w;
            }
            wait_lgkm<0>(); bar();
            STAMP(l * 24 + 6);
            {
                u32x4 im[5];
                sfor<0, 5>([&](auto T) { im[T] = *(LAS u32x4*)(lds + L_IMG + T * FRAG + voff); });
                const u32x4 da = *(LAS u32x4*)(lds + L_IMG + 5 * FRAG + g * 16), db = *(LAS u32x4*)(lds + L_IMG + 5 * FRAG + 64 + g * 16);
                const char* obase = ws + WS_O + (size_t)l * XT_BYTES + (size_t)mt_a * KSD * FRAG;
                u32x2 t2p[2];
                sfor<0, 2>([&](auto M) {
                    constexpr int m = M;
                    f32x4 t0, t1, t2, dA, dB;
                    attn_mfmas(t0, t1, t2, dA, dB, im, da, db, qa[m], qb[m]);
                    const float ia = 1.f / (dA[0] + 1e-6f), ib = 1.f / (dB[0] + 1e-6f);
                    const float i0 = g < 3 ? ia : ib, i1 = g < 2 ? ia : ib;
                    const u32x4 w = {pk_bf16(t0[0] * i0, t0[1] * i0), pk_bf16(t0[2] * i0, t0[3] * i0), pk_bf16(t1[0] * i1, t1[1] * i1), pk_bf16(t1[2] * i1, t1[3] * i1)};
                    const int G = 5 * cg + g;
                    st_v(obase, (m * KSD + (G >> 2)) * FRAG + (G & 3) * 256 + r * 16, w);
                    t2p[m] = (u32x2){pk_bf16(t2[0] * ib, t2[1] * ib), pk_bf16(t2[2] * ib, t2[3] * ib)};
                });
                const auto s0 = __builtin_amdgcn_permlane16_swap(t2p[0][0], t2p[1][0], false, false);
                const auto s1 = __builtin_amdgcn_permlane16_swap(t2p[0][1], t2p[1][1], false, false);
                const u32x4 w = {s0[0], s1[0], s0[1], s1[1]};
                const int G = 5 * cg + 4;
                if (g < 2) st_v(obase, (g * KSD + (G >> 2)) * FRAG + (G & 3) * 256 + r * 16, w);
            }
            wait_vm<0>(); bar();
            if (tid == 0) signal(ws, C_O + l * 16 + rp);
            fresh_ptrs();
            sfor<0, 4>([&](auto C) { w_chunk(IC<V_P3 + C>{}); });
            STAMP(l * 24 + 7);
        }
        auto proj = [&](auto PH) {
            constexpr int ph = PH;
            constexpr int NS = ph == 2 ? 5 : 14, KS = ph == 2 ? KSD : KSF;
            const char* abase = ws + (ph == 2 ? WS_O + (size_t)l * XT_BYTES : WS_H + (size_t)l * HT_BYTES) + (size_t)(rt * 4 * KS + wid * NS) * FRAG;
            if constexpr (ph == 2) fresh_lanes();
            const char* asrc[4]; const char* alast[4];
            sfor<0, 4>([&](auto M) { asrc[M] = abase + (size_t)(((wid + M) & 3) * KS) * FRAG; pins(asrc[M]);
                alast[M] = asrc[M] + (size_t)((ph == 4 && wid >= 2) ? 12 : NS - 1) * FRAG; });
            fresh_ptrs();
            constexpr int AW = 5;
            bf16x8 A2[5][4];
            auto& A = *(ph == 2 ? &A2 : &AH);
            f32x4 acc[4][5];
            unsigned flagv;
            auto emit = [&](auto K) {
                constexpr Op o = ph == 2 ? SP2.op[K] : SP4.op[K];
                if constexpr (o.kind == K_A) {
                    if constexpr (o.a == NS - 1) ld_x<0>(A[o.a % AW][o.b], alast[o.b], voff);
                    else ld_x<(o.a % 4) * FRAG>(A[o.a % AW][o.b], asrc[o.b] + (size_t)(o.a / 4) * 4 * FRAG, voff);
                }
                else if constexpr (o.kind == K_W) w_op(IC<o.a>{}, IC<o.b>{});
                else if constexpr (o.a == X_WP) ld_w<w4_reg(o.b), (o.b % 4) * FRAG>(wP4 + (size_t)(o.b / 4) * 4 * FRAG, voff);
                else if constexpr (o.a == X_FLAG) ld_v1(flagv, ws + WS_CTL + (size_t)(C_HB + l * 16 + (rt >> 1)) * 256, 0u);
            };
            constexpr int first = ph == 2 ? 0 : 3;
            constexpr int npro = ph == 2 ? SP2.before(0) : SP4.before(0);
            sfor<first, npro>(emit);
            unsigned xw[3];
            sfor<0, 3>([&](auto M) { xw[M] = lds0 + L_XCH + ((((wid + M + 1) & 3) * 3 + M) * 5) * FRAG + voff; });
            sfor<0, NS * 20>([&](auto I) {
                constexpr int i = I, s = i / 20;
                constexpr bool last = s == NS - 1;
                constexpr int n = last ? i % 5 : (i % 20) / 4, m = last ? (((i % 20) / 5 + 1) & 3) : i % 4;
                if constexpr (i % 20 == 0) {
                    constexpr int wa = ph == 2 ? SP2.wait(i, K_A, s, 3) : SP4.wait(i, K_A, s, 3);
                    constexpr int ww = ph == 2 ? 63 : SP4.wait(i, K_X, X_WP, s * 5 + 4);
                    constexpr int w0 = (ph == 4 && s < 4) ? SP4.before(i) : 63;
                    wait_vm<cmin(cmin(wa, ww), w0)>();
                }
                if constexpr (ph == 4 && i == 48) {
                    wait_vm<SP4.wait(i, K_X, X_HST, 2)>(); bar();
                    if (tid == 0) signal(ws, C_HB + l * 16 + rp);
                }
                if constexpr (ph == 4 && i == 140) {
                    wait_vm<SP4.wait(i, K_X, X_FLAG, 0)>();
                    pin(flagv);
                    if (__builtin_amdgcn_readfirstlane(flagv) < 16) spin_ge(ws, C_HB + l * 16 + (rt >> 1), 16u);
                }
                if constexpr (ph == 2) mma_w<s == 0, w2_reg(s * 5 + n)>(acc[m][n], A[s % AW][m]);
                else mma_w<s == 0, w4_reg(s * 5 + n)>(acc[m][n], A[s % AW][m]);
                if constexpr (ph == 2) sfor<SP2.before(i), SP2.before(i + 1)>(emit); else sfor<SP4.before(i), SP4.before(i + 1)>(emit);
                if constexpr (last && i % 20 >= 10) { constexpr int q = i % 20 - 10, t = q / 5 + 1; ds_wr<(q % 5) * FRAG>(xw[t - 1], acc[t][q % 5]); }
            });
            mma_settle();
            sfor<0, 4>([&](auto M) { sfor<0, 5>([&](auto N) { mma_fence(acc[M][N]); }); });
            STAMP(l * 24 + (ph == 2 ? 9 : 17));
            fresh_lanes();
            sfor<0, 5>([&](auto N) { ds_wr<N * FRAG>(xw[2], acc[3][N]); });
            wait_lgkm<0>(); bar();
            f32x4 fin[5];
            sfor<0, 5>([&](auto N) {
                const unsigned o = L_XCH + (wid * 15 + N) * FRAG + voff;
                fin[N] = (acc[0][N] + *(LAS f32x4*)(lds + o)) + (*(LAS f32x4*)(lds + o + 5 * FRAG) + *(LAS f32x4*)(lds + o + 10 * FRAG));
            });
            auto fin_pair = [&](const f32x4& lo, const f32x4& hi, const u32x4& x) {
                u32x4 w;
                w[0] = pk_bf16(lo[0] + bf_lo(x[0]), lo[1] + bf_hi(x[0])); w[1] = pk_bf16(lo[2] + bf_lo(x[1]), lo[3] + bf_hi(x[1]));
                w[2] = pk_bf16(hi[0] + bf_lo(x[2]), hi[1] + bf_hi(x[2])); w[3] = pk_bf16(hi[2] + bf_lo(x[3]), hi[3] + bf_hi(x[3]));
                return w;
            };
            resA = fin_pair(fin[0], fin[1], resA); resB = fin_pair(fin[2], fin[3], resB);
            resH = (u32x2){pk_bf16(fin[4][0] + bf_lo(resH[0]), fin[4][1] + bf_hi(resH[0])), pk_bf16(fin[4][2] + bf_lo(resH[1]), fin[4][3] + bf_hi(resH[1]))};
            if (ph == 4 && l == 1) {
                char* row = a.out + (size_t)(mtf * 16 + r) * 1280;
                *(GAS u32x4*)(row + tA * 64 + g * 16) = resA;
                *(GAS u32x4*)(row + tA * 64 + 64 + g * 16) = resB;
                *(GAS u32x2*)(row + tH * 64 + odd * 32 + g * 8) = resH;
            } else {
                const int xo = 2 * l + (ph == 2 ? 1 : 2);
                const char* xb = ws + WS_X + (size_t)xo * XT_BYTES + (size_t)mtf * KSD * FRAG;
                st_v(xb + tA * FRAG, voff, resA); st_v(xb + (tA + 1) * FRAG, voff, resB); st_v2(xb, hoff_f(), resH);
                float s1 = 0.f, s2 = 0.f;
                auto accum = [&](unsigned u) { const float lo = bf_lo(u), hi = bf_hi(u); s1 += lo + hi; s2 += lo * lo + hi * hi; };
                sfor<0, 4>([&](auto J) { constexpr int j = J; accum(resA[j]); accum(resB[j]); });
                accum(resH[0]); accum(resH[1]);
                s1 = xg_sum(s1); s2 = xg_sum(s2);
                if (g == 0) st_v2(ws + WS_ST + (size_t)xo * ST_BYTES + (size_t)mtf * 16 * 64, r * 64 + c8 * 8, (u32x2){__float_as_uint(s1), __float_as_uint(s2)});
                wait_vm<0>(); bar();
                if (tid == 0) signal(ws, (ph == 2 ? C_X1 + l * 16 : C_X2) + (rt >> 1));
                fresh_ptrs();
                if constexpr (ph == 2) { w_chunk(IC<V_P3 + 4>{}); w_chunk(IC<V_P3 + 5>{});
                    sfor<0, W4_G>([&](auto J) { ld_w<w4_reg(J), (J % 4) * FRAG>(wP4 + (size_t)(J / 4) * 4 * FRAG, voff); }); }
                else sfor<0, 6>([&](auto V) { w_chunk(IC<V_END + V>{}); });
            }
        };
        if (wid == 0) spin_ge(ws, C_O + l * 16 + (rt >> 1), 16u);
        bar();
        STAMP(l * 24 + 8);
        proj(IC<2>{});
        STAMP(l * 24 + 10);
        {
            const char* xsrc = ws + WS_X + (size_t)(2 * l + 1) * XT_BYTES + (size_t)mt_a * KSD * FRAG;
            const char* stsrc = ws + WS_ST + (size_t)(2 * l + 1) * ST_BYTES + (size_t)mt_a * 16 * 64;
            const char* hb = ws + WS_H + (size_t)l * HT_BYTES + (size_t)mt_a * KSF * FRAG;
            if (wid == 0) spin_ge(ws, C_X1 + l * 16 + rp, 16u);
            wait_vm<0>(); bar();
            STAMP(l * 24 + 11);
            fresh_lanes(); fresh_ptrs(); pins(xsrc);
            f32x4 accA[2][8], accB[2][6];
            u32x4 stp[2]; unsigned flagv;
            const char* hsrc[4];
            sfor<0, 4>([&](auto M) { hsrc[M] = ws + WS_H + (size_t)l * HT_BYTES + (size_t)((rt * 4 + ((wid + M) & 3)) * KSF + wid * 14) * FRAG; });
            auto emit = [&](auto K) {
                constexpr Op o = SP3.op[K];
                if constexpr (o.kind == K_A) ld_a<xa_reg(o.b, o.a), (o.a % 4) * FRAG>(xsrc + (size_t)(o.b * KSD + (o.a / 4) * 4) * FRAG, voff);
                else if constexpr (o.kind == K_W) w_op(IC<o.a>{}, IC<o.b>{});
                else if constexpr (o.a == X_STAT) ld_v<o.b * FRAG>(stp[o.b], stsrc, r * 64 + g * 16);
                else if constexpr (o.a == X_WP) ld_w<w4_reg(o.b), (o.b % 4) * FRAG>(wP4 + (size_t)(o.b / 4) * 4 * FRAG, voff);
                else if constexpr (o.a == X_FLAG) ld_v1(flagv, ws + WS_CTL + (size_t)(C_HA + l * 16 + (rt >> 1)) * 256, 0u);
                else if constexpr (o.a == X_HA) ld_x<(o.b >> 2) * FRAG>(AH[o.b >> 2][o.b & 3], hsrc[o.b & 3], voff);
            };
            sfor<0, SP3.before(0)>(emit);
            float rstd[2], mrs[2];
            constexpr int L = 6, R = 7;
            bf16x8 B[R];
            f32x4 cs[8];
            auto rd = [&](auto I, auto LIM) { constexpr int i = I; if constexpr (i < LIM) ds_rd_at<slot_of(V_P3 + i / 20) + (i % 20) * FRAG>(B[i % R], rdb, rdb_hi); };
            sfor<0, L>([&](auto I) { rd(I, IC<160>{}); });
            wait_lgkm<L - 1>();
            sfor<0, 160>([&](auto I) {
                constexpr int i = I, ks = i / 8, n = i % 8;
                if constexpr (n == 0) wait_vm<SP3.wait(i, K_A, ks, 1)>();
                if constexpr (i == 40) {
                    wait_vm<SP3.wait(i, K_X, X_STAT, 1)>();
                    pin(stp[0]); pin(stp[1]);
                    row_stats(stp[0], rstd[0], mrs[0]); row_stats(stp[1], rstd[1], mrs[1]);
                }
                if constexpr (i == 60) { pin(rstd[0]); pin(rstd[1]); pin(mrs[0]); pin(mrs[1]); }
                if constexpr (i % 20 == 20 - L && i / 20 + 1 >= 2 && i / 20 + 1 < 8) { wait_vm<SP3.wait(i, K_W, V_P3 + i / 20 + 1, 4)>(); bar(); }
                if constexpr (i == 154) sfor<0, 8>([&](auto N) { cs[N] = *(LAS f32x4*)(lds + L_CSG + (N * 16 + 4 * g) * 4); });
                mma<ks == 0, xa_reg(0, ks)>(accA[0][n], B[i % R]);
                rd(IC<i + L>{}, IC<160>{});
                mma<ks == 0, xa_reg(1, ks)>(accA[1][n], B[i % R]);
                sfor<SP3.before(i), (i == 159 ? SP3.find(K_X, X_HST, 0) : SP3.before(i + 1))>(emit);
                if constexpr (i + 1 < 160) wait_lgkm<cmin(L - 1, 158 - i)>();
            });
            mma_settle();
            sfor<0, 8>([&](auto N) { mma_fence(accA[0][N]); mma_fence(accA[1][N]); });
            STAMP(l * 24 + 12);
            auto h_tile = [&](const f32x4& ga, const f32x4& ua, const f32x4& cgv, const f32x4& cuv, int m) {
                const f32x4 h = (ga * rstd[m] - cgv * mrs[m]) * (ua * rstd[m] - cuv * mrs[m]);
                return (u32x2){pk_bf16(h[0], h[1]), pk_bf16(h[2], h[3])};
            };
            sfor<0, 2>([&](auto P) {
                sfor<0, 2>([&](auto M) {
                    constexpr int p = P, m = M;
                    const u32x2 lo = h_tile(accA[m][4 * p], accA[m][4 * p + 1], cs[4 * p], cs[4 * p + 1], m), hi = h_tile(accA[m][4 * p + 2], accA[m][4 * p + 3], cs[4 * p + 2], cs[4 * p + 3], m);
                    st_v(hb + (size_t)(m * KSF + h_pos_a(cg, p)) * FRAG, voff, (u32x4){lo[0], lo[1], hi[0], hi[1]});
                });
            });
            STAMP(l * 24 + 13);
            fresh_ptrs();
            wait_vm<SP3.wait(160, K_W, V_P3 + 8, 4)>(); wait_lgkm<0>(); bar();
            sfor<160, 160 + L>([&](auto I) { rd(I, IC<280>{}); });
            wait_lgkm<L - 1>();
            sfor<160, 280>([&](auto I) {
                constexpr int i = I, ks = (i - 160) / 6, n = (i - 160) % 6;
                if constexpr (i % 20 == 20 - L && i / 20 + 1 < 14) {
                    constexpr int wv = SP3.wait(i, K_W, V_P3 + i / 20 + 1, 4);
                    wait_vm<wv>(); bar();
                }
                if constexpr (i == K_HASIG) { wait_vm<SP3.wait(i, K_X, X_HST, 3)>(); bar(); if (tid == 0) signal(ws, C_HA + l * 16 + rp); }
                if constexpr (i == K_HACHK) {
                    wait_vm<SP3.wait(i, K_X, X_FLAG, 0)>();
                    pin(flagv);
                    if (__builtin_amdgcn_readfirstlane(flagv) < 16) spin_ge(ws, C_HA + l * 16 + (rt >> 1), 16u);
                    sfor<0, 4>([&](auto M) { pins(hsrc[M]); });
                }
                mma<ks == 0, xa_reg(0, ks)>(accB[0][n], B[i % R]);
                rd(IC<i + L>{}, IC<280>{});
                mma<ks == 0, xa_reg(1, ks)>(accB[1][n], B[i % R]);
                sfor<SP3.before(i), SP3.before(i + 1)>(emit);
                if constexpr (i + 1 < 280) wait_lgkm<cmin(L - 1, 278 - i)>();
            });
            sfor<0, 6>([&](auto N) { cs[N] = *(LAS f32x4*)(lds + L_CSG + ((8 + N) * 16 + 4 * g) * 4); });
            mma_settle();
            sfor<0, 6>([&](auto N) { mma_fence(accB[0][N]); mma_fence(accB[1][N]); });
            STAMP(l * 24 + 14);
            fresh_lanes();
            u32x2 t6[2];
            sfor<0, 2>([&](auto M) {
                constexpr int m = M;
                const u32x2 lo = h_tile(accB[m][0], accB[m][1], cs[0], cs[1], m), hi = h_tile(accB[m][2], accB[m][3], cs[2], cs[3], m);
                st_v(hb + (size_t)(m * KSF + h_pos_b(cg)) * FRAG, voff, (u32x4){lo[0], lo[1], hi[0], hi[1]});
                t6[m] = h_tile(accB[m][4], accB[m][5], cs[4], cs[5], m);
            });
            {
                const auto s0 = __builtin_amdgcn_permlane16_swap(t6[0][0], t6[1][0], false, false);
                const auto s1 = __builtin_amdgcn_permlane16_swap(t6[0][1], t6[1][1], false, false);
                const u32x4 w = {s0[0], s1[0], s0[1], s1[1]};
                const char* dst = cg < 12 ? hb + (size_t)h_pos_b(16 + (cg >> 1)) * FRAG : ws + WS_DUMMY;
                const unsigned off = cg < 12 ? (g & 1) * KSF * FRAG + (2 * (cg & 1) + (g >> 1)) * 256 + r * 16 : (blockIdx.x * 4 + wid) * FRAG + voff;
                st_v(dst, off, w);
            }
            STAMP(l * 24 + 15);
        }
        proj(IC<4>{});
        STAMP(l * 24 + 18);
    }
}

extern "C" void kernel_launch(void* const* d_in, const int* in_sizes, int n_in, void* d_out, int out_size, void* d_ws, size_t ws_size, hipStream_t stream) {
    char* ws = (char*)d_ws;
    (void)hipFuncSetAttribute((const void*)prep_kernel, hipFuncAttributeMaxDynamicSharedMemorySize, PREP_LDS);
    (void)hipFuncSetAttribute((const void*)main_kernel, hipFuncAttributeMaxDynamicSharedMemorySize, LDS_BYTES);
    PrepArgs pa{(const char*)d_in[0], (const char*)d_in[1], (const char*)d_in[2], (const char*)d_in[4], (const char*)d_in[5], (const char*)d_in[6], ws, (const char*)d_out};
    hipLaunchKernelGGL(prep_kernel, dim3(PREP_UNITS), dim3(256), PREP_LDS, stream, pa);
    MainArgs ma{ws, (char*)d_out};
    hipLaunchKernelGGL(main_kernel, dim3(256), dim3(256), LDS_BYTES, stream, ma);
}
```

```cpp
#include <hip/hip_runtime.h>
#include <hip/hip_bf16.h>
#include <type_traits>

#define LAS __attribute__((address_space(3)))
#define GAS __attribute__((address_space(1)))
#define DI __device__ __forceinline__
typedef short bf16x8 __attribute__((ext_vector_type(8)));
typedef short s16x4 __attribute__((ext_vector_type(4)));
typedef float f32x4 __attribute__((ext_vector_type(4)));
typedef float f32x2 __attribute__((ext_vector_type(2)));
typedef unsigned u32x4 __attribute__((ext_vector_type(4)));
typedef unsigned u32x2 __attribute__((ext_vector_type(2)));
typedef __bf16 bf16x2_n __attribute__((ext_vector_type(2)));
typedef GAS unsigned gu32;

constexpr int SEQ = 2048, DM = 640;
constexpr int KSD = 20, KSF = 56, NMT = 128;
constexpr int FRAG = 1024;

constexpr size_t WS_CTL = 0;
constexpr int CTL_LINES = 256;
constexpr int C_TMO = 0, C_KV = 1, C_O = 33, C_X1 = 65, C_HA = 97, C_HB = 129, C_X2 = 161;
constexpr size_t CTL_BYTES = (size_t)CTL_LINES * 256;
constexpr size_t WS_WQKV = CTL_BYTES;
constexpr size_t WS_WO = WS_WQKV + (size_t)16 * 160 * FRAG;
constexpr size_t WS_WGU = WS_WO + (size_t)8 * 100 * FRAG;
constexpr size_t WS_WD = WS_WGU + (size_t)16 * 280 * FRAG;
constexpr size_t WS_CSQ = WS_WD + (size_t)8 * 280 * FRAG;
constexpr size_t WS_CSG = WS_CSQ + (size_t)16 * 128 * 20 * 4;
constexpr size_t XT_BYTES = (size_t)NMT * KSD * FRAG;
constexpr size_t WS_X = WS_CSG + (size_t)16 * 224 * 20 * 4;
constexpr size_t ST_BYTES = (size_t)SEQ * 64;
constexpr size_t WS_ST = WS_X + 2 * XT_BYTES;
constexpr size_t WS_O = WS_ST + 2 * ST_BYTES;
constexpr size_t HT_BYTES = (size_t)NMT * KSF * FRAG;
constexpr size_t WS_H = WS_O + XT_BYTES;
constexpr size_t KVP_SLOT = 3584;
constexpr size_t WS_KVP = WS_H + HT_BYTES;
constexpr size_t WS_DUMMY = WS_KVP + (size_t)2 * 16 * 16 * KVP_SLOT;
constexpr size_t WS_END = WS_DUMMY + (size_t)1024 * FRAG;

template <int I, int N, class F> DI void sfor(F&& f) { if constexpr (I < N) { f(std::integral_constant<int, I>{}); sfor<I + 1, N>(f); } }
template <int V> using IC = std::integral_constant<int, V>;

DI unsigned pk_bf16(float lo, float hi) { const bf16x2_n v = __builtin_convertvector((f32x2){lo, hi}, bf16x2_n); return __builtin_bit_cast(unsigned, v); }
DI float bf_lo(unsigned u) { return __uint_as_float(u << 16); }
DI float bf_hi(unsigned u) { return __uint_as_float(u & 0xffff0000u); }
DI float xg_sum(float v) {
    const unsigned u = __float_as_uint(v);
    const auto a = __builtin_amdgcn_permlane16_swap(u, u, false, false);
    const float s = __uint_as_float(a[0]) + __uint_as_float(a[1]);
    const unsigned w = __float_as_uint(s);
    const auto b = __builtin_amdgcn_permlane32_swap(w, w, false, false);
    return __uint_as_float(b[0]) + __uint_as_float(b[1]);
}

template <int N> DI void wait_vm() { static_assert(N >= 0 && N < 64, "vmcnt range"); asm volatile("s_waitcnt vmcnt(%0)" ::"i"(N) : "memory"); }
template <int N> DI void wait_lgkm() { static_assert(N >= 0 && N < 16, "lgkmcnt range"); asm volatile("s_waitcnt lgkmcnt(%0)" ::"i"(N) : "memory"); }
DI void bar() { asm volatile("s_barrier" ::: "memory"); }
template <int OFF> DI void ds_rd(bf16x8& d, unsigned addr) { asm volatile("ds_read_b128 %0, %1 offset:%2" : "=v"(d) : "v"(addr), "i"(OFF) : "memory"); }
template <int T> DI void ds_rd_at(bf16x8& d, unsigned lo, unsigned hi) { ds_rd<T % 65536>(d, T < 65536 ? lo : hi); }
template <int R, int OFF> DI void ld_a(const char* sb, unsigned voff) { asm volatile("global_load_dwordx4 a[%c0:%c1], %2, %3 offset:%4 sc1" ::"i"(R), "i"(R + 3), "v"(voff), "s"(sb), "i"(OFF) : "memory"); }
template <int R, int OFF> DI void ld_w(const char* sb, unsigned voff) { asm volatile("global_load_dwordx4 a[%c0:%c1], %2, %3 offset:%4" ::"i"(R), "i"(R + 3), "v"(voff), "s"(sb), "i"(OFF) : "memory"); }
typedef unsigned long long u64;
template <int OFF> DI void ld_v(u32x4& d, const char* sb, unsigned voff) { u64 t; asm volatile("s_mov_b64 %1, %3\n\tglobal_load_dwordx4 %0, %2, %1 offset:%4 sc1" : "=v"(d), "=&s"(t) : "v"(voff), "s"(sb), "i"(OFF) : "memory"); }
DI void ld_v2(u32x2& d, const char* sb, unsigned voff) { u64 t; asm volatile("s_mov_b64 %1, %3\n\tglobal_load_dwordx2 %0, %2, %1 sc1" : "=v"(d), "=&s"(t) : "v"(voff), "s"(sb) : "memory"); }
template <int OFF = 0> DI void ld_v1(unsigned& d, const char* sb, unsigned voff) { u64 t; asm volatile("s_mov_b64 %1, %3\n\tglobal_load_dword %0, %2, %1 offset:%4 sc1" : "=v"(d), "=&s"(t) : "v"(voff), "s"(sb), "i"(OFF) : "memory"); }
DI void st_v(const char* sb, unsigned voff, const u32x4& v) { u64 t; asm volatile("s_mov_b64 %0, %3\n\tglobal_store_dwordx4 %1, %2, %0 sc1\n\ts_nop 1" : "=&s"(t) : "v"(voff), "v"(v), "s"(sb) : "memory"); }
DI void st_v2(const char* sb, unsigned voff, const u32x2& v) { u64 t; asm volatile("s_mov_b64 %0, %3\n\tglobal_store_dwordx2 %1, %2, %0 sc1\n\ts_nop 1" : "=&s"(t) : "v"(voff), "v"(v), "s"(sb) : "memory"); }
DI void set_m0(unsigned lds) { asm volatile("s_mov_b32 m0, %0\n\ts_nop 0" ::"s"(lds) : "memory"); }
template <int OFF> DI void dma(const char* sb, unsigned voff) { asm volatile("global_load_lds_dwordx4 %0, %1 offset:%2" ::"v"(voff), "s"(sb), "i"(OFF) : "memory"); }
template <int P> DI void dma_piece(const char* sb, unsigned lds, unsigned voff) {
    if constexpr (P % 4 == 0) { set_m0(lds + P * FRAG); }
    dma<(P % 4) * FRAG>(sb + (P / 4) * 4 * FRAG, voff);
}
template <bool FIRST, int R> DI void mma(f32x4& c, const bf16x8& w) {
    if constexpr (FIRST) asm volatile("v_mfma_f32_16x16x32_bf16 %0, %1, a[%c2:%c3], 0" : "=v"(c) : "v"(w), "i"(R), "i"(R + 3));
    else asm volatile("v_mfma_f32_16x16x32_bf16 %0, %1, a[%c2:%c3], %0" : "+v"(c) : "v"(w), "i"(R), "i"(R + 3));
}
template <bool FIRST, int R> DI void mma_t(f32x4& c, const bf16x8& w) {
    if constexpr (FIRST) asm volatile("v_mfma_f32_16x16x32_bf16 %0, a[%c2:%c3], %1, 0" : "=v"(c) : "v"(w), "i"(R), "i"(R + 3));
    else asm volatile("v_mfma_f32_16x16x32_bf16 %0, a[%c2:%c3], %1, %0" : "+v"(c) : "v"(w), "i"(R), "i"(R + 3));
}
DI void mma_settle() { asm volatile("s_nop 7\n\ts_nop 7" ::: "memory"); }
DI void mma_fence(f32x4& c) { asm volatile("" : "+v"(c)); }
template <class T> DI void pins(T& x) { asm volatile("" : "+s"(x)); }
DI void pins_done() { asm volatile("s_nop 4" ::: "memory"); }
template <class T> DI void pin(T& x) { asm volatile("" : "+v"(x)); }

DI void kv_mfmas(f32x4 (&kv)[8], const u32x4 (&op)[5], const u32x4& v2) {
    asm volatile("s_nop 1\n\t"
                 "v_mfma_f32_16x16x32_bf16 %0, %8, %11, 0\n\tv_mfma_f32_16x16x32_bf16 %1, %8, %12, 0\n\tv_mfma_f32_16x16x32_bf16 %2, %8, %13, 0\n\t"
                 "v_mfma_f32_16x16x32_bf16 %3, %9, %11, 0\n\tv_mfma_f32_16x16x32_bf16 %4, %9, %12, 0\n\tv_mfma_f32_16x16x32_bf16 %5, %9, %13, 0\n\t"
                 "v_mfma_f32_16x16x32_bf16 %6, %10, %12, 0\n\tv_mfma_f32_16x16x32_bf16 %7, %10, %13, 0\n\ts_nop 7"
                 : "=&v"(kv[0]), "=&v"(kv[1]), "=&v"(kv[2]), "=&v"(kv[3]), "=&v"(kv[4]), "=&v"(kv[5]), "=&v"(kv[6]), "=&v"(kv[7])
                 : "v"(op[0]), "v"(op[1]), "v"(op[2]), "v"(op[3]), "v"(op[4]), "v"(v2));
}
DI void attn_mfmas(f32x4& t0, f32x4& t1, f32x4& t2, f32x4& dA, f32x4& dB, const u32x4 (&im)[5], const u32x4& da, const u32x4& db, const u32x4& qa, const u32x4& qb) {
    asm volatile("s_nop 1\n\t"
                 "v_mfma_f32_16x16x32_bf16 %3, %10, %12, 0\n\tv_mfma_f32_16x16x32_bf16 %4, %11, %13, 0\n\t"
                 "v_mfma_f32_16x16x32_bf16 %0, %5, %12, 0\n\tv_mfma_f32_16x16x32_bf16 %1, %7, %12, 0\n\tv_mfma_f32_16x16x32_bf16 %2, %9, %13, 0\n\t"
                 "v_mfma_f32_16x16x32_bf16 %0, %6, %13, %0\n\tv_mfma_f32_16x16x32_bf16 %1, %8, %13, %1\n\ts_nop 7"
                 : "=&v"(t0), "=&v"(t1), "=&v"(t2), "=&v"(dA), "=&v"(dB)
                 : "v"(im[0]), "v"(im[1]), "v"(im[2]), "v"(im[3]), "v"(im[4]), "v"(da), "v"(db), "v"(qa), "v"(qb));
}
DI float phi(float t) { return (t > 0.f ? t + 1.f : __expf(t)) + 1e-6f; }

__host__ __device__ constexpr int h_tile_base(int f) { return f <= 12 ? 7 * f : 84 + 6 * (f - 12); }
__host__ __device__ constexpr int h_pos_a(int f, int p) { return ((2 * f + p) >> 3) * 14 + ((2 * f + p) & 7); }
__host__ __device__ constexpr int h_pos_b(int ib) { return ib < 12 ? (ib / 6) * 14 + 8 + ib % 6 : (2 + (ib - 12) / 5) * 14 + 8 + (ib - 12) % 5; }

DI void map16x16(int b, int& r, int& c) { const int x = b & 7, q = b >> 3; r = (x >> 2) * 8 + (q & 7); c = (x & 3) * 4 + (q >> 3); }
DI void map32x8(int b, int& r, int& c) { const int x = b & 7, q = b >> 3; r = (x >> 1) * 8 + (q & 7); c = (x & 1) * 4 + (q >> 3); }

constexpr int PU_Q = 80, PU_G = 160, PU_X = 128, PU_D = 108, PU_O = 40, PREP_UNITS = PU_Q + PU_G + PU_X + PU_D + PU_O;
constexpr int PAGE_STRIDE = 256 << 10;
constexpr int PREP_IMG = 32 * 1024, PREP_LDS = PREP_IMG + 1024;
struct PrepArgs { const char *x, *wqkv, *wo, *wg, *wu, *wd; char* ws; const char* out; };

template <int N> DI void tr_frags(bf16x8 (&fr)[N], const unsigned (&addr)[N], unsigned rs) {
    s16x4 lo[N], hi[N];
#pragma unroll
    for (int i = 0; i < N; ++i) asm volatile("ds_read_b64_tr_b16 %0, %2\n\tds_read_b64_tr_b16 %1, %3" : "=&v"(lo[i]), "=&v"(hi[i]) : "v"(addr[i]), "v"(addr[i] + 4 * rs) : "memory");
    asm volatile("s_waitcnt lgkmcnt(0)" ::: "memory");
#pragma unroll
    for (int i = 0; i < N; ++i) { asm volatile("" : "+v"(lo[i]), "+v"(hi[i])); fr[i] = __builtin_shufflevector(lo[i], hi[i], 0, 1, 2, 3, 4, 5, 6, 7); }
}
DI void prep_st16(char* dst, const u32x4& v) { asm volatile("global_store_dwordx4 %0, %1, off sc1\n\ts_nop 1" ::"v"(dst), "v"(v) : "memory"); }
DI void prep_store(char* dst, const bf16x8& fr, int lane) { prep_st16(dst + lane * 16, __builtin_bit_cast(u32x4, fr)); }
DI void prep_colsum(float* dst, const bf16x8& fr, int lane) {
    const bf16x8 ones = {0x3f80, 0x3f80, 0x3f80, 0x3f80, 0x3f80, 0x3f80, 0x3f80, 0x3f80};
    const f32x4 z = {0.f, 0.f, 0.f, 0.f};
    const f32x4 d = __builtin_amdgcn_mfma_f32_16x16x32_bf16(fr, ones, z, 0, 0, 0);
    if ((lane & 15) == 0) prep_st16((char*)(dst + (lane >> 4) * 4), __builtin_bit_cast(u32x4, d));
}
DI int wswz(int row) { return 2 * ((row & 3) + 4 * ((row >> 3) & 1)); }
DI unsigned wimg_addr(unsigned img, unsigned rs, int row, unsigned off) { return img + row * rs + ((((off >> 4) ^ wswz(row)) << 4) | (off & 15)); }
constexpr unsigned MAIN_CODE_BYTES = 41 * 1024;
DI const char* main_code() {
    unsigned long long p;
    asm volatile("s_getpc_b64 s[98:99]\n\ts_add_u32 s98, s98, _Z11main_kernel8MainArgs@rel32@lo+4\n\ts_addc_u32 s99, s99, _Z11main_kernel8MainArgs@rel32@hi+12\n\t"
                 "s_mov_b64 %0, s[98:99]" : "=s"(p) :: "s98", "s99", "scc");
    return (const char*)p;
}
constexpr unsigned PREP_CODE_BYTES = 6 * 1024;
DI const char* prep_code() {
    unsigned long long p;
    asm volatile("s_getpc_b64 s[98:99]\n\ts_add_u32 s98, s98, _Z11prep_kernel8PrepArgs@rel32@lo+4\n\ts_addc_u32 s99, s99, _Z11prep_kernel8PrepArgs@rel32@hi+12\n\t"
                 "s_mov_b64 %0, s[98:99]" : "=s"(p) :: "s98", "s99", "scc");
    return (const char*)p;
}
DI void prep_dma(const char* src, LAS char* dst) { __builtin_amdgcn_global_load_lds((const GAS unsigned*)src, (LAS unsigned*)dst, 16, 0, 0); }

__global__ __launch_bounds__(256, 4) void prep_kernel(PrepArgs a) {
    extern __shared__ __attribute__((aligned(16))) char shm[];
    const int tid = threadIdx.x, wid = __builtin_amdgcn_readfirstlane(tid >> 6), lane = tid & 63;
    int u = blockIdx.x;
    const int g = lane >> 4, q = (lane & 15) >> 2, p = lane & 3, c16 = lane & 15;
    LAS char* img = (LAS char*)shm;
    const unsigned imgaddr = (unsigned)(size_t)shm;
    const bf16x8 zero = {0, 0, 0, 0, 0, 0, 0, 0};
    if (wid == 0) {
        constexpr int NP = (int)((WS_END + PAGE_STRIDE - 1) / PAGE_STRIDE), NO = (SEQ * DM * 2 + PAGE_STRIDE - 1) / PAGE_STRIDE, PER = (NP + NO + 63) / 64;
        const int q = blockIdx.x >> 3;
#pragma unroll
        for (int it = 0; it < (PER + 63) / 64; ++it) {
            const int k = it * 64 + lane, pg = q * PER + k;
            if (k < PER && pg < NP + NO) { const char* p = pg < NP ? a.ws + (size_t)pg * PAGE_STRIDE : a.out + (size_t)(pg - NP) * PAGE_STRIDE;
                __builtin_amdgcn_global_load_lds((const GAS unsigned*)p, (LAS unsigned*)(img + PREP_IMG), 4, 0, 0); }
        }
    }
    if (wid == 3) {
        const unsigned off = ((blockIdx.x >> 3) & 15) * 1024 + lane * 16;
        if (off + 16 <= PREP_CODE_BYTES) __builtin_amdgcn_global_load_lds((const GAS unsigned*)(prep_code() + off), (LAS unsigned*)(img + PREP_IMG), 16, 0, 0);
    }
    if (wid == 1 || wid == 2) {
        const unsigned size = MAIN_CODE_BYTES; const char* code = main_code();
        const unsigned off = (((blockIdx.x >> 3) % 24) * 2 + wid - 1) * 1024 + lane * 16;
        if (off + 16 <= size) __builtin_amdgcn_global_load_lds((const GAS unsigned*)(code + off), (LAS unsigned*)(img + PREP_IMG), 16, 0, 0);
    }
    if (u < PU_Q) {
        const int ks = u >> 2, qq = u & 3;
#pragma unroll 1
        for (int it = 0; it < 8; ++it) {
            const int i = (it * 4 + wid) * 64 + lane, row = i >> 6, cc = (i & 63) ^ wswz(row), seg = cc / 20, c = cc % 20;
            prep_dma(a.wqkv + (size_t)(ks * 32 + row) * 3840 + qq * 320 + (cc < 60 ? seg * 1280 + c * 16 : 0), img + (it * 4 + wid) * 1024);
        }
        wait_vm<0>(); __syncthreads();
        const unsigned rs = 1024;
        const int hp = qq * 4 + wid, row = 8 * g + q;
        const unsigned qo = wid * 80, ko = qo + 320, vo = qo + 640;
        const unsigned of[8] = {ko + 8 * p, ko + 32 + 8 * p, p < 2 ? ko + 64 + 8 * p : vo + 64 + 8 * (p - 2), vo + 8 * p, vo + 32 + 8 * p, qo + 8 * p, qo + 40 + 8 * p, qo + (p == 0 ? 32 : 72)};
        unsigned ad[8];
#pragma unroll
        for (int nt = 0; nt < 8; ++nt) ad[nt] = wimg_addr(imgaddr, rs, row, of[nt]);
        bf16x8 fr[8];
        tr_frags<8>(fr, ad, rs);
        if (c16 >= 8) fr[7] = zero;
        char* w1 = a.ws + WS_WQKV + (size_t)hp * 160 * FRAG + (size_t)ks * 5 * FRAG;
        char* w2 = a.ws + WS_WQKV + (size_t)hp * 160 * FRAG + (size_t)(100 + ks * 3) * FRAG;
        float* cs = (float*)(a.ws + WS_CSQ) + (hp * 20 + ks) * 128;
#pragma unroll
        for (int nt = 0; nt < 8; ++nt) { prep_store(nt < 5 ? w1 + nt * FRAG : w2 + (nt - 5) * FRAG, fr[nt], lane); prep_colsum(cs + nt * 16, fr[nt], lane); }
        return;
    }
    u -= PU_Q;
    if (u < PU_G) {
        const int ks = u >> 3, fq = (u >> 1) & 3, up = u & 1;
        const char* w = up ? a.wu : a.wg;
#pragma unroll 1
        for (int it = 0; it < 8; ++it) {
            const int i = (it * 4 + wid) * 64 + lane, row = i >> 6, cc = (i & 63) ^ wswz(row);
            prep_dma(w + (size_t)(ks * 32 + row) * 3456 + fq * 896 + (cc < (fq == 3 ? 48 : 56) ? cc * 16 : 0), img + (it * 4 + wid) * 1024);
        }
        wait_vm<0>(); __syncthreads();
        const int f = fq * 4 + wid, nt7 = f < 12;
        const unsigned rs = 1024, colo = (h_tile_base(f) - h_tile_base(fq * 4)) * 32;
        unsigned ad[7];
#pragma unroll
        for (int j = 0; j < 7; ++j) ad[j] = wimg_addr(imgaddr, rs, 8 * g + q, colo + 2 * (j < 6 ? 32 * (j >> 1) + 8 * p + 4 * (j & 1) : (nt7 ? 96 : 0) + 4 * p));
        bf16x8 fr[7];
        tr_frags<7>(fr, ad, rs);
        if (!nt7) fr[6] = zero;
        char* da = a.ws + WS_WGU + (size_t)f * 280 * FRAG + (size_t)(ks * 8 + up) * FRAG;
        char* db = a.ws + WS_WGU + (size_t)f * 280 * FRAG + (size_t)(160 + ks * 6 + up) * FRAG;
        float* cs = (float*)(a.ws + WS_CSG) + (f * 20 + ks) * 224 + up * 16;
#pragma unroll
        for (int j = 0; j < 7; ++j) { prep_store(j < 4 ? da + 2 * j * FRAG : db + 2 * (j - 4) * FRAG, fr[j], lane); prep_colsum(cs + j * 32, fr[j], lane); }
        return;
    }
    u -= PU_G;
    if (u < PU_X) {
        if (u == 0) __hip_atomic_store((gu32*)(a.ws + WS_CTL + tid * 256), 0u, __ATOMIC_RELAXED, __HIP_MEMORY_SCOPE_AGENT);
        const int mt = u, r = lane & 15;
        const char* src = a.x + (size_t)mt * 16 * 1280;
#pragma unroll
        for (int it = 0; it < 5; ++it) { const int i = (wid * 5 + it) * 64 + lane, row = i / 80, c = (i % 80) ^ row; prep_dma(src + row * 1280 + c * 16, img + (wid * 5 + it) * 1024); }
        wait_vm<0>(); __syncthreads();
        char* dst = a.ws + WS_X + (size_t)(mt * KSD + wid * 5) * FRAG + lane * 16;
        const bf16x8 ones = {0x3f80, 0x3f80, 0x3f80, 0x3f80, 0x3f80, 0x3f80, 0x3f80, 0x3f80};
        f32x4 s1 = {0.f, 0.f, 0.f, 0.f}, s2 = {0.f, 0.f, 0.f, 0.f};
#pragma unroll
        for (int j = 0; j < 5; ++j) {
            const u32x4 v = *(LAS u32x4*)(img + r * 1280 + (((wid * 5 + j) * 4 + g) ^ r) * 16);
            prep_st16(dst + j * FRAG, v);
            const bf16x8 xf = __builtin_bit_cast(bf16x8, v);
            s1 = __builtin_amdgcn_mfma_f32_16x16x32_bf16(xf, ones, s1, 0, 0, 0);
            s2 = __builtin_amdgcn_mfma_f32_16x16x32_bf16(xf, xf, s2, 0, 0, 0);
        }
        const int j = r & 3;
        const float a1 = j == 0 ? s1[0] : j == 1 ? s1[1] : j == 2 ? s1[2] : s1[3], a2 = j == 0 ? s2[0] : j == 1 ? s2[1] : j == 2 ? s2[2] : s2[3];
        LAS f32x2* red = (LAS f32x2*)(img + 24 * 1024);
        if ((r >> 2) == g) red[wid * 16 + r] = (f32x2){a1, a2};
        __syncthreads();
        if (wid == 0) {
            const f32x2 t = (red[r] + red[16 + r]) + (red[32 + r] + red[48 + r]);
            prep_st16(a.ws + WS_ST + (size_t)(mt * 16 + r) * 64 + g * 16, g == 0 ? (u32x4){__float_as_uint(t[0]), __float_as_uint(t[1]), 0u, 0u} : (u32x4){0u, 0u, 0u, 0u});
        }
        return;
    }
    u -= PU_X;
    {
        const bool down = u < PU_D;
        const int uu = down ? u : u - PU_D, v = uu >> 1, hf = uu & 1;
        int kq, s, row_lo, row_hi;
        if (!down) { kq = v / 5; s = v % 5; row_lo = v * 32; row_hi = row_lo + 16; }
        else if (v < 32) { kq = v >> 3; s = v & 7; row_lo = 16 * (h_tile_base(v >> 1) + 2 * (v & 1)); row_hi = row_lo + 16; }
        else {
            const int ib = v - 32, pos = h_pos_b(ib); kq = pos / 14; s = pos % 14;
            if (ib < 16) { row_lo = 16 * (h_tile_base(ib) + 4); row_hi = row_lo + 16; }
            else { const int f0 = 2 * (ib - 16); row_lo = 16 * (h_tile_base(f0) + 6); row_hi = 16 * (h_tile_base(f0 + 1) + 6); }
        }
        const char* w = down ? a.wd : a.wo;
#pragma unroll 1
        for (int it = 0; it < 6; ++it) {
            const int i = (it * 4 + wid) * 64 + lane, row = i / 48, c = (i % 48) ^ wswz(row);
            const int wrow = row < 16 ? row_lo + row : row_hi + row - 16;
            prep_dma(w + (size_t)wrow * 1280 + hf * 640 + (c < 40 ? c * 16 : 0), img + (it * 4 + wid) * 1024);
        }
        wait_vm<0>(); __syncthreads();
        const unsigned rs = 768;
        const int c8 = hf * 4 + wid;
        const int pa = (c8 & 1) ? 16 : 0, hfo = (c8 & 1) ? 0 : 64;
        unsigned ad[5];
#pragma unroll
        for (int nt = 0; nt < 5; ++nt) ad[nt] = wimg_addr(imgaddr, rs, 8 * g + q, wid * 160 + 2 * (nt < 4 ? pa + 32 * (nt >> 1) + 8 * p + 4 * (nt & 1) : hfo + 4 * p));
        bf16x8 fr[5];
        tr_frags<5>(fr, ad, rs);
        char* dst = a.ws + (down ? WS_WD + (size_t)((c8 * 4 + kq) * 14 + s) * 5 * FRAG : WS_WO + (size_t)((c8 * 4 + kq) * 5 + s) * 5 * FRAG);
#pragma unroll
        for (int nt = 0; nt < 5; ++nt) prep_store(dst + nt * FRAG, fr[nt], lane);
        if (down && kq >= 2 && s == 12) {
#pragma unroll
            for (int nt = 0; nt < 5; ++nt) prep_store(dst + (5 + nt) * FRAG, zero, lane);
        }
    }
}

enum { K_A = 0, K_W = 1, K_X = 2 };
struct Op { int pos, kind, a, b; };
template <int N> struct Sched {
    Op op[N] = {}; int n = 0;
    constexpr void add(int pos, int kind, int a, int b) { op[n] = Op{pos, kind, a, b}; ++n; }
    constexpr void sort() { for (int i = 1; i < n; ++i) { const Op t = op[i]; int j = i - 1; while (j >= 0 && op[j].pos > t.pos) { op[j + 1] = op[j]; --j; } op[j + 1] = t; } }
    constexpr int before(int pos) const { int c = 0; for (int i = 0; i < n; ++i) c += op[i].pos < pos; return c; }
    constexpr int find(int kind, int a, int b) const { for (int i = 0; i < n; ++i) if (op[i].kind == kind && op[i].a == a && op[i].b == b) return i; return -1; }
    constexpr int wait(int pos, int kind, int a, int b) const { const int i = find(kind, a, b); if (i < 0) return 63; const int w = before(pos) - i - 1; return w < 0 ? -1 : (w > 63 ? 63 : w); }
};
constexpr int cmin(int a, int b) { return a < b ? a : b; }

constexpr int HA_SIG = 184, HA_FLAG = 236, HA_CHK = 254;
constexpr int NSLOT = 6, SLOT = 20 * FRAG;
constexpr int V_P1 = 0, V_P3 = 12, V_END = 30;
constexpr int slot_of(int v) { return (v % NSLOT) * SLOT; }

constexpr int xa_reg(int m, int ks) { return (m * 20 + ks) * 4; }
constexpr int w2_reg(int j) { return j < 24 ? 160 + 4 * j : xa_reg(0, 0); }
constexpr int w4_reg(int j) { return j < 24 ? 160 + 4 * j : (j < 64 ? xa_reg((j - 24) & 1, (j - 24) >> 1) : 160 + 4 * (j - 64)); }

enum { X_STAT = 0, X_KVST = 1, X_HST = 2, X_FLAG = 3, X_CS = 4, X_RES = 5, X_WP = 6, X_HA = 7 };

template <bool FIRST> constexpr Sched<160> make_p1() {
    Sched<160> s;
    if (FIRST) {
        for (int p = 0; p < 5; ++p) s.add(-1, K_W, 0, p);
        for (int k = 0; k < 2; ++k) { s.add(-1, K_A, k, 0); s.add(-1, K_A, k, 1); }
        for (int p = 0; p < 5; ++p) s.add(-1, K_W, 1, p);
        s.add(-1, K_X, X_STAT, 0); s.add(-1, K_X, X_STAT, 1);
        int pos = 0;
        for (int k = 2; k < 20; ++k) {
            s.add(pos++, K_A, k, 0); s.add(pos++, K_A, k, 1);
            if (k == 3 || k == 7 || k == 11) for (int p = 0; p < 5; ++p) s.add(pos++, K_W, 2 + (k - 3) / 4, p);
        }
        for (int p = 0; p < 5; ++p) s.add(pos++, K_W, 5, p);
        pos = 61;
        for (int c = 0; c < 2; ++c) for (int p = 0; p < 5; ++p) { s.add(pos, K_W, 6 + c, p); pos += 3; }
        for (int i = 0; i < 40; ++i) s.add(57 + i, K_X, X_CS, i);
        for (int i = 0; i < 3; ++i) s.add(92 + 2 * i, K_X, X_RES, i);
    } else {
        for (int k = 0; k < 5; ++k) { s.add(-1, K_A, k, 0); s.add(-1, K_A, k, 1); }
        s.add(-1, K_X, X_STAT, 0); s.add(-1, K_X, X_STAT, 1);
        for (int k = 5; k < 8; ++k) { s.add(4 * (k - 5), K_A, k, 0); s.add(4 * (k - 5) + 2, K_A, k, 1); }
        for (int k = 8; k < 20; ++k) { s.add(5 * (k - 8) + 12, K_A, k, 0); s.add(5 * (k - 8) + 14, K_A, k, 1); }
        const int w6[5] = {64, 69, 74, 78, 82}, w7[5] = {86, 89, 92, 95, 98};
        for (int p = 0; p < 5; ++p) { s.add(w6[p], K_W, 6, p); s.add(w7[p], K_W, 7, p); }
    }
    s.sort(); return s;
}
constexpr Sched<128> make_p1b() {
    Sched<128> s;
    s.add(-1, K_X, X_KVST, 0);
    for (int j = 0; j < 25; ++j) s.add(j < 21 ? 6 + 2 * j : 8 + 2 * j, K_X, X_WP, j);
    s.add(47, K_X, X_FLAG, 0);
    s.sort(); return s;
}
constexpr Sched<128> make_p2() {
    Sched<128> s;
    for (int k = 0; k < 3; ++k) for (int m = 0; m < 4; ++m) s.add(-1, K_A, k, m);
    for (int k = 3; k < 5; ++k) for (int m = 0; m < 4; ++m) s.add(((k - 3) * 4 + m) * 4 + 1, K_A, k, m);
    s.sort(); return s;
}
constexpr int W4_G = 12, W4_A = 24, W4_B = 54;
constexpr int P4_AW = 5;
constexpr Sched<192> make_p3() {
    Sched<192> s;
    for (int k = 0; k < 4; ++k) { s.add(-1, K_A, k, 0); s.add(-1, K_A, k, 1); }
    s.add(-1, K_X, X_STAT, 0); s.add(-1, K_X, X_STAT, 1);
    for (int k = 4; k < 6; ++k) { s.add(4 * (k - 4), K_A, k, 0); s.add(4 * (k - 4) + 2, K_A, k, 1); }
    for (int k = 6; k < 20; ++k) { s.add(8 * (k - 6) + 9, K_A, k, 0); s.add(8 * (k - 6) + 12, K_A, k, 1); }
    for (int c = 2; c < 10; ++c) for (int p = 0; p < 5; ++p) { int pos = 20 * c - 6 + 1 + p * 4; if (c == 8 && pos < 160) pos = 160; s.add(pos, K_W, V_P3 + c + 4, p); }
    for (int j = W4_G; j < W4_A; ++j) s.add(111 + 4 * (j - W4_G), K_X, X_WP, j);
    for (int i = 0; i < 4; ++i) s.add(159, K_X, X_HST, i);
    for (int j = W4_A; j < W4_B; ++j) s.add(166 + 3 * (j - W4_A), K_X, X_WP, j);
    s.add(HA_FLAG, K_X, X_FLAG, 0);
    for (int j = 0; j < 16; ++j) s.add(HA_CHK + 1 + (j * (279 - HA_CHK - 1)) / 16, K_X, X_HA, j);
    s.sort(); return s;
}
constexpr Sched<160> make_p4() {
    Sched<160> s;
    for (int i = 0; i < 3; ++i) s.add(-1, K_X, X_HST, i);
    for (int k = 4; k < 8; ++k) for (int m = 0; m < 4; ++m) s.add(20 * (k - 4) + 1 + 5 * m, K_A, k, m);
    for (int j = W4_B; j < 70; ++j) { const int t = j - W4_B; s.add(t < 4 ? 20 * t + 19 : 82 + (t - 4) * 5, K_X, X_WP, j); }
    s.add(112, K_X, X_FLAG, 0);
    for (int k = 8; k < 14; ++k) for (int m = 0; m < 4; ++m) { const int t = (k - 8) * 4 + m; s.add(t < 8 ? 140 + 2 * t : 160 + 20 * (k - 10) + 4 * m, K_A, k, m); }
    s.sort(); return s;
}
template <bool FIRST> constexpr Sched<160> SP1 = make_p1<FIRST>();
constexpr auto SP1B = make_p1b();
constexpr auto SP3 = make_p3();
constexpr auto SP2 = make_p2();
constexpr auto SP4 = make_p4();

constexpr int L_RING = 0, L_SCR = NSLOT * SLOT, L_IMG = L_SCR + 8 * FRAG, L_XCH = 4 * SLOT, L_TAB = L_SCR + 32 * FRAG, L_CSQ = L_TAB + 1024, L_CSG = L_CSQ + 512,
              LDS_BYTES = L_CSG + 1024;
struct MainArgs { char* ws; char* out; };

DI void row_stats(const u32x4& p, float& rstd, float& mrs) {
    float s = __uint_as_float(p[0]) + __uint_as_float(p[2]), s2 = __uint_as_float(p[1]) + __uint_as_float(p[3]);
    s = xg_sum(s); s2 = xg_sum(s2);
    const float mean = s * (1.f / DM), var = fmaxf(s2 * (1.f / DM) - mean * mean, 0.f);
    rstd = __builtin_amdgcn_rsqf(var + 1e-5f);
    mrs = mean * rstd;
}
DI void spin_ge(const char* ws, int line, unsigned target) {
    gu32* p = (gu32*)(ws + WS_CTL + (size_t)line * 256);
    gu32* tmo = (gu32*)(ws + WS_CTL + C_TMO * 256);
    unsigned spins = 0;
    while ((unsigned)__builtin_amdgcn_readfirstlane(__hip_atomic_load(p, __ATOMIC_RELAXED, __HIP_MEMORY_SCOPE_AGENT)) < target) {
        __builtin_amdgcn_s_sleep(1);
        ++spins;
        if ((spins & 1023u) == 0u && __builtin_amdgcn_readfirstlane(__hip_atomic_load(tmo, __ATOMIC_RELAXED, __HIP_MEMORY_SCOPE_AGENT))) break;
        if (spins > (1u << 17)) { __hip_atomic_store(tmo, 1u, __ATOMIC_RELAXED, __HIP_MEMORY_SCOPE_AGENT); break; }
    }
    asm volatile("" ::: "memory");
}
DI void signal(char* ws, int line) {
    u64 t; const char* p = ws + WS_CTL + (size_t)line * 256;
    asm volatile("s_mov_b64 %0, %1\n\tglobal_atomic_add %2, %3, %0" : "=&s"(t) : "s"(p), "v"(0u), "v"(1u) : "memory");
}
template <bool FIRST, int R> DI void mma_w(f32x4& c, const bf16x8& x) {
    if constexpr (FIRST) asm volatile("v_mfma_f32_16x16x32_bf16 %0, a[%c2:%c3], %1, 0" : "=v"(c) : "v"(x), "i"(R), "i"(R + 3));
    else asm volatile("v_mfma_f32_16x16x32_bf16 %0, a[%c2:%c3], %1, %0" : "+v"(c) : "v"(x), "i"(R), "i"(R + 3));
}
template <int OFF> DI void ld_x(bf16x8& d, const char* sb, unsigned voff) { asm volatile("global_load_dwordx4 %0, %1, %2 offset:%3 sc1" : "=v"(d) : "v"(voff), "s"(sb), "i"(OFF) : "memory"); }
template <int OFF> DI void ds_wr(unsigned addr, const f32x4& v) { asm volatile("ds_write_b128 %0, %1 offset:%2" ::"v"(addr), "v"(v), "i"(OFF) : "memory"); }

__global__ __launch_bounds__(256, 1) void main_kernel(MainArgs a) {
    extern __shared__ __attribute__((aligned(16))) char shm[];
    const unsigned lds0 = (unsigned)(size_t)shm;
    LAS char* lds = (LAS char*)shm;
    char* const ws = a.ws;
    int tid = threadIdx.x;
    const int wid = __builtin_amdgcn_readfirstlane(tid >> 6);
    int g = (tid & 63) >> 4, r = tid & 15;
    unsigned voff = (tid & 63) * 16;
    int rp, cg; map16x16(blockIdx.x, rp, cg);
    int rt, c8; map32x8(blockIdx.x, rt, c8);
    const int mt_a = rp * 8 + wid * 2;
    const int mtf = rt * 4 + wid, odd = c8 & 1;
    const int tA = odd ? (5 * c8 + 1) / 2 : 5 * c8 / 2, tH = odd ? (5 * c8 - 1) / 2 : 5 * c8 / 2 + 2;
    auto hoff_f = [&]() { return (unsigned)(tH * FRAG + (odd * 2 + (g >> 1)) * 256 + r * 16 + (g & 1) * 8); };
    asm volatile("" ::: "a0", "a255");

    const char* wP1 = ws + WS_WQKV + (size_t)(cg * 160 + wid * 5) * FRAG;
    const char* wP3 = ws + WS_WGU + (size_t)(cg * 280 + wid * 5) * FRAG;
    const char* wP2 = ws + WS_WO + (size_t)((c8 * 4 + wid) * 25) * FRAG;
    const char* wP4 = ws + WS_WD + (size_t)((c8 * 4 + wid) * 70) * FRAG;
    unsigned wdst = lds0 + L_RING + wid * 5 * FRAG;
    auto fresh_ptrs = [&]() { pins(wP1); pins(wP2); pins(wP3); pins(wP4); pins(wdst); pins_done(); };
    unsigned rdb, rdb_hi;
    auto fresh_lanes = [&]() { pin(tid); g = (tid & 63) >> 4; r = tid & 15; voff = (tid & 63) * 16; rdb = lds0 + L_RING + voff; rdb_hi = rdb + 65536; };
    auto w_op = [&](auto V, auto P) {
        constexpr int v0 = V, v = v0 % V_END, p = P;
        const char* src = v < V_P3 ? wP1 + (size_t)(v - V_P1) * SLOT : wP3 + (size_t)(v - V_P3) * SLOT;
        dma_piece<p>(src, wdst + slot_of(v), voff);
    };
    auto w_chunk = [&](auto V) { sfor<0, 5>([&](auto P) { w_op(V, P); }); };

    unsigned kvo, kv_red, kv_img;
    {
        const int t = tid < 210 ? tid : 209; const bool z = t >= 200; const int hd0 = t / 100, v = t % 100;
        const int dg = z ? t - 200 : hd0 * 5 + v / 20, e = z ? 0 : hd0 * 20 + v % 20;
        const int dt = dg >> 2, gg = dg & 3, et = z ? 2 : e >> 4, cc = z ? 0 : (e < 32 ? e & 15 : e - 24);
        kvo = t * 16;
        kv_red = L_SCR + (dt == 2 ? 5 + et : dt * 3 + et) * FRAG + (gg * 16 + cc) * 16;
        const int hd = dg >= 5, dl = dg - hd * 5, ig = dl < 4 ? dl : hd, hb = dl < 4 ? 0 : 8;
        const int T = e >= 32 ? 2 : (e >> 2) & 1, c = e >= 32 ? e - 32 : (e >> 3) * 4 + (e & 3);
        kv_img = z ? L_IMG + 5 * FRAG + hd * 64 + ig * 16 + hb : L_IMG + (T == 2 ? 4 : T * 2 + hd) * FRAG + (ig * 16 + c) * 16 + hb;
        pin(kvo); pin(kv_red); pin(kv_img);
    }
    u32x4 resA, resB; u32x2 resH;
    float csk[5]; f32x4 csq[3];
    bf16x8 AH[P4_AW][4];

    for (int l = 0; l < 2; ++l) {
        {
            const char* xsrc = ws + WS_X + (size_t)mt_a * KSD * FRAG;
            const char* stsrc = ws + WS_ST + (size_t)mt_a * 16 * 64;
            char* kvp = ws + WS_KVP + (size_t)((l * 16 + cg) * 16) * KVP_SLOT;
            if (l > 0) { if (wid == 0) spin_ge(ws, C_X2 + rp, 16u); wait_vm<0>(); bar(); }
            fresh_lanes(); pins(xsrc); fresh_ptrs();
            f32x4 acc1[2][5], acc2[2][3];
            float rstd[2], mrs[2];
            f32x4 st[2][2];
            auto pass1 = [&](auto FST) {
                constexpr bool first = FST;
                u32x4 stp[2]; unsigned cq[20], cgp[20];
                const char* csqb = ws + WS_CSQ + (size_t)cg * 20 * 512; const char* csgb = ws + WS_CSG + (size_t)cg * 20 * 896; const char* xr = ws + WS_X + (size_t)mtf * KSD * FRAG;
                pins(csqb); pins(csgb); pins(xr); pins_done();
                auto emit = [&](auto K) {
                    constexpr Op o = SP1<first>.op[K];
                    if constexpr (o.kind == K_A) ld_a<xa_reg(o.b, o.a), (o.a % 4) * FRAG>(xsrc + (size_t)(o.b * KSD + (o.a / 4) * 4) * FRAG, voff);
                    else if constexpr (o.kind == K_W) w_op(IC<o.a>{}, IC<o.b>{});
                    else if constexpr (o.a == X_STAT) ld_v<o.b * FRAG>(stp[o.b], stsrc, r * 64 + g * 16);
                    else if constexpr (o.a == X_CS) {
                        if constexpr (o.b < 20) ld_v1<(o.b % 8) * 512>(cq[o.b], csqb + (o.b / 8) * 4096, (tid & 127) * 4);
                        else ld_v1<((o.b - 20) % 4) * 896>(cgp[o.b - 20], csgb + ((o.b - 20) / 4) * 3584, (tid < 224 ? tid : 0) * 4);
                    } else if constexpr (o.a == X_RES) {
                        if constexpr (o.b == 0) ld_v<0>(resA, xr + tA * FRAG, voff); else if constexpr (o.b == 1) ld_v<FRAG>(resB, xr + tA * FRAG, voff); else ld_v2(resH, xr, hoff_f());
                    }
                };
                sfor<0, SP1<first>.before(0)>(emit);
                constexpr int L = 4, R = 5;
                bf16x8 B[R];
                auto rd = [&](auto I) { constexpr int i = I; if constexpr (i < 100) ds_rd_at<slot_of(V_P1 + i / 20) + (i % 20) * FRAG>(B[i % R], rdb, rdb_hi); };
                if constexpr (first) { wait_vm<SP1<first>.wait(0, K_W, 0, 4)>(); bar(); }
                sfor<0, L>(rd);
                wait_lgkm<L - 1>();
                sfor<0, 100>([&](auto I) {
                    constexpr int i = I, ks = i / 5, n = i % 5;
                    if constexpr (n == 0) wait_vm<SP1<first>.wait(i, K_A, ks, 1)>();
                    if constexpr (i == 30) {
                        wait_vm<SP1<first>.wait(i, K_X, X_STAT, 1)>();
                        pin(stp[0]); pin(stp[1]);
                        row_stats(stp[0], rstd[0], mrs[0]); row_stats(stp[1], rstd[1], mrs[1]);
                        if (g == 0) {
                            *(LAS f32x2*)(lds + L_TAB + (wid * 32 + r) * 8) = (f32x2){rstd[0], mrs[0]};
                            *(LAS f32x2*)(lds + L_TAB + (wid * 32 + 16 + r) * 8) = (f32x2){rstd[1], mrs[1]};
                        }
                    }
                    if constexpr (i == 50) {
                        sfor<0, 2>([&](auto M) { st[M][0] = *(LAS f32x4*)(lds + L_TAB + (wid * 32 + M * 16 + 4 * g) * 8); st[M][1] = *(LAS f32x4*)(lds + L_TAB + (wid * 32 + M * 16 + 4 * g) * 8 + 16); });
                    }
                    if constexpr (i == 70) { pin(st[0][0]); pin(st[0][1]); pin(st[1][0]); pin(st[1][1]); pin(rstd[0]); pin(rstd[1]); pin(mrs[0]); pin(mrs[1]); }
                    if constexpr (i % 20 == 20 - L && i < 80 && first) { wait_vm<SP1<first>.wait(i, K_W, i / 20 + 1, 4)>(); bar(); }
                    if constexpr (i == 60) bar();
                    mma_t<ks == 0, xa_reg(0, ks)>(acc1[0][n], B[i % R]);
                    rd(IC<i + L>{});
                    mma_t<ks == 0, xa_reg(1, ks)>(acc1[1][n], B[i % R]);
                    sfor<SP1<first>.before(i), SP1<first>.before(i + 1)>(emit);
                    if constexpr (i + 1 < 100) wait_lgkm<cmin(L - 1, 98 - i)>();
                });
                if constexpr (first) {
                    wait_vm<cmin(SP1<first>.n - SP1<first>.find(K_X, X_RES, 2) - 1, SP1<first>.n - SP1<first>.find(K_X, X_CS, 39) - 1)>();
                    float sq = 0.f, sg = 0.f;
                    sfor<0, 20>([&](auto J) { constexpr int j = J; pin(cq[j]); pin(cgp[j]); sq += __uint_as_float(cq[j]); sg += __uint_as_float(cgp[j]); });
                    pin(resA); pin(resB); pin(resH);
                    if (tid < 128) *(LAS float*)(lds + L_CSQ + tid * 4) = sq;
                    if (tid < 224) *(LAS float*)(lds + L_CSG + tid * 4) = sg;
                    wait_lgkm<0>(); bar();
                    sfor<0, 5>([&](auto N) { csk[N] = *(LAS float*)(lds + L_CSQ + (N * 16 + r) * 4); });
                    sfor<0, 3>([&](auto N) { csq[N] = *(LAS f32x4*)(lds + L_CSQ + ((5 + N) * 16 + 4 * g) * 4); });
                }
            };
            if (l == 0) pass1(std::true_type{}); else pass1(std::false_type{});
            mma_settle();
            sfor<0, 5>([&](auto N) { mma_fence(acc1[0][N]); mma_fence(acc1[1][N]); });
            fresh_lanes();
            {
                u32x4 op[5];
                sfor<0, 5>([&](auto N) {
                    constexpr int n = N;
                    float v[2][4];
                    sfor<0, 2>([&](auto M) {
                        sfor<0, 4>([&](auto J) { constexpr int m = M, j = J;
                            float t = acc1[m][n][j] * st[m][j >> 1][(j & 1) * 2] - st[m][j >> 1][(j & 1) * 2 + 1] * csk[n];
                            if constexpr (n < 2) t = phi(t); else if constexpr (n == 2) t = r < 8 ? phi(t) : t;
                            v[m][j] = t; });
                    });
                    op[n] = (u32x4){pk_bf16(v[0][0], v[0][1]), pk_bf16(v[0][2], v[0][3]), pk_bf16(v[1][0], v[1][1]), pk_bf16(v[1][2], v[1][3])};
                });
                u32x4 v2 = op[2];
                if (r < 8) v2 = (u32x4){0x3f803f80u, 0x3f803f80u, 0x3f803f80u, 0x3f803f80u};
                f32x4 kv[8];
                kv_mfmas(kv, op, v2);
                sfor<0, 8>([&](auto T) { *(LAS f32x4*)(lds + L_SCR + (wid * 8 + T) * FRAG + voff) = kv[T]; });
            }
            wait_lgkm<0>(); bar();
            {
                const f32x4 sum = (*(LAS f32x4*)(lds + kv_red) + *(LAS f32x4*)(lds + kv_red + 8 * FRAG)) + (*(LAS f32x4*)(lds + kv_red + 16 * FRAG) + *(LAS f32x4*)(lds + kv_red + 24 * FRAG));
                st_v(kvp + rp * KVP_SLOT, kvo, __builtin_bit_cast(u32x4, sum));
            }
            fresh_ptrs();
            unsigned kvflag = 0;
            {
                constexpr int R2 = 6, L2 = 4;
                bf16x8 B2[R2];
                auto rd = [&](auto I) { constexpr int i = I; if constexpr (i < 60) ds_rd_at<slot_of(V_P1 + 5 + i / 20) + (i % 20) * FRAG>(B2[i % R2], rdb, rdb_hi); };
                auto emit = [&](auto K) { constexpr Op o = SP1B.op[K];
                    if constexpr (o.a == X_WP) ld_w<w2_reg(o.b), (o.b % 4) * FRAG>(wP2 + (size_t)(o.b / 4) * 4 * FRAG, voff);
                    else if constexpr (o.a == X_FLAG) ld_v1(kvflag, ws + WS_CTL + (size_t)(C_KV + l * 16 + cg) * 256, 0u); };
                sfor<0, L2>(rd);
                wait_lgkm<L2 - 1>();
                sfor<0, 60>([&](auto I) {
                    constexpr int i = I, ks = i / 3, n = i % 3;
                    if constexpr (i == 20 - L2) {
                        wait_vm<SP1B.wait(i, K_X, X_KVST, 0)>(); bar();
                        if (tid == 0) signal(ws, C_KV + l * 16 + cg);
                        *(LAS u32x4*)(lds + L_IMG + tid * 16) = (u32x4){0u, 0u, 0u, 0u};
                        if (tid < 128) *(LAS u32x4*)(lds + L_IMG + 4096 + tid * 16) = (u32x4){0u, 0u, 0u, 0u};
                    }
                    mma<ks == 0, xa_reg(0, ks)>(acc2[0][n], B2[i % R2]);
                    rd(IC<i + L2>{});
                    mma<ks == 0, xa_reg(1, ks)>(acc2[1][n], B2[i % R2]);
                    sfor<SP1B.before(i), SP1B.before(i + 1)>(emit);
                    if constexpr (i + 1 < 60) wait_lgkm<cmin(L2 - 1, 58 - i)>();
                });
            }
            mma_settle();
            sfor<0, 3>([&](auto N) { mma_fence(acc2[0][N]); mma_fence(acc2[1][N]); });
            fresh_lanes();
            wait_vm<SP1B.n - SP1B.find(K_X, X_FLAG, 0) - 1>();
            pin(kvflag);
            if (__builtin_amdgcn_readfirstlane(kvflag) < 16) spin_ge(ws, C_KV + l * 16 + cg, 16u);
            wait_lgkm<0>(); bar();
            u32x4 pv[16];
            sfor<0, 16>([&](auto I) { ld_v<0>(pv[I], kvp + I * KVP_SLOT, kvo); });
            u32x4 qa[2], qb[2];
            sfor<0, 2>([&](auto M) {
                constexpr int m = M;
                unsigned pk[3][2];
                sfor<0, 3>([&](auto N) {
                    constexpr int n = N;
                    float v[4];
                    sfor<0, 4>([&](auto J) { constexpr int j = J; v[j] = phi(acc2[m][n][j] * rstd[m] - mrs[m] * csq[n][j]); });
                    pk[n][0] = pk_bf16(v[0], v[1]); pk[n][1] = pk_bf16(v[2], v[3]);
                });
                qa[m] = (u32x4){pk[0][0], pk[0][1], pk[2][0], pk[2][1]};
                qb[m] = (u32x4){pk[1][0], pk[1][1], pk[2][0], pk[2][1]};
            });
            pin(qa[0]); pin(qa[1]); pin(qb[0]); pin(qb[1]);
            {
                wait_vm<0>();
                sfor<0, 16>([&](auto I) { pin(pv[I]); });
                f32x4 s0 = __builtin_bit_cast(f32x4, pv[0]) + __builtin_bit_cast(f32x4, pv[1]), s1 = __builtin_bit_cast(f32x4, pv[2]) + __builtin_bit_cast(f32x4, pv[3]);
#pragma unroll
                for (int i = 4; i < 16; i += 2) { s0 += __builtin_bit_cast(f32x4, pv[i]); s1 += __builtin_bit_cast(f32x4, pv[i + 1]); }
                const f32x4 s = s0 + s1;
                const u32x2 w = {pk_bf16(s[0], s[1]), pk_bf16(s[2], s[3])};
                *(LAS u32x2*)(lds + kv_img) = w;
            }
            wait_lgkm<0>(); bar();
            {
                u32x4 im[5];
                sfor<0, 5>([&](auto T) { im[T] = *(LAS u32x4*)(lds + L_IMG + T * FRAG + voff); });
                const u32x4 da = *(LAS u32x4*)(lds + L_IMG + 5 * FRAG + g * 16), db = *(LAS u32x4*)(lds + L_IMG + 5 * FRAG + 64 + g * 16);
                const char* obase = ws + WS_O + (size_t)mt_a * KSD * FRAG;
                u32x2 t2p[2];
                sfor<0, 2>([&](auto M) {
                    constexpr int m = M;
                    f32x4 t0, t1, t2, dA, dB;
                    attn_mfmas(t0, t1, t2, dA, dB, im, da, db, qa[m], qb[m]);
                    const float ia = __builtin_amdgcn_rcpf(dA[0] + 1e-6f), ib = __builtin_amdgcn_rcpf(dB[0] + 1e-6f);
                    const float i0 = g < 3 ? ia : ib, i1 = g < 2 ? ia : ib;
                    const u32x4 w = {pk_bf16(t0[0] * i0, t0[1] * i0), pk_bf16(t0[2] * i0, t0[3] * i0), pk_bf16(t1[0] * i1, t1[1] * i1), pk_bf16(t1[2] * i1, t1[3] * i1)};
                    const int G = 5 * cg + g;
                    st_v(obase, (m * KSD + (G >> 2)) * FRAG + (G & 3) * 256 + r * 16, w);
                    t2p[m] = (u32x2){pk_bf16(t2[0] * ib, t2[1] * ib), pk_bf16(t2[2] * ib, t2[3] * ib)};
                });
                const auto s0 = __builtin_amdgcn_permlane16_swap(t2p[0][0], t2p[1][0], false, false);
                const auto s1 = __builtin_amdgcn_permlane16_swap(t2p[0][1], t2p[1][1], false, false);
                const u32x4 w = {s0[0], s1[0], s0[1], s1[1]};
                const int G = 5 * cg + 4;
                if (g < 2) st_v(obase, (g * KSD + (G >> 2)) * FRAG + (G & 3) * 256 + r * 16, w);
            }
            wait_vm<0>(); bar();
            if (tid == 0) signal(ws, C_O + l * 16 + rp);
            fresh_ptrs();
            sfor<0, 4>([&](auto C) { w_chunk(IC<V_P3 + C>{}); });
        }
        auto proj = [&](auto PH) {
            constexpr int ph = PH;
            constexpr int NS = ph == 2 ? 5 : 14, KS = ph == 2 ? KSD : KSF;
            const char* abase = ws + (ph == 2 ? WS_O : WS_H) + (size_t)(rt * 4 * KS + wid * NS) * FRAG;
            if constexpr (ph == 2) fresh_lanes();
            const char* asrc[4]; const char* alast[4];
            sfor<0, 4>([&](auto M) { asrc[M] = abase + (size_t)(((wid + M) & 3) * KS) * FRAG; pins(asrc[M]);
                alast[M] = asrc[M] + (size_t)((ph == 4 && wid >= 2) ? 12 : NS - 1) * FRAG; });
            fresh_ptrs();
            constexpr int AW = 5;
            bf16x8 A2[5][4];
            auto& A = *(ph == 2 ? &A2 : &AH);
            f32x4 acc[4][5];
            unsigned flagv;
            auto emit = [&](auto K) {
                constexpr Op o = ph == 2 ? SP2.op[K] : SP4.op[K];
                if constexpr (o.kind == K_A) {
                    if constexpr (o.a == NS - 1) ld_x<0>(A[o.a % AW][o.b], alast[o.b], voff);
                    else ld_x<(o.a % 4) * FRAG>(A[o.a % AW][o.b], asrc[o.b] + (size_t)(o.a / 4) * 4 * FRAG, voff);
                }
                else if constexpr (o.kind == K_W) w_op(IC<o.a>{}, IC<o.b>{});
                else if constexpr (o.a == X_WP) ld_w<w4_reg(o.b), (o.b % 4) * FRAG>(wP4 + (size_t)(o.b / 4) * 4 * FRAG, voff);
                else if constexpr (o.a == X_FLAG) ld_v1(flagv, ws + WS_CTL + (size_t)(C_HB + l * 16 + (rt >> 1)) * 256, 0u);
            };
            constexpr int first = ph == 2 ? 0 : 3;
            constexpr int npro = ph == 2 ? SP2.before(0) : SP4.before(0);
            sfor<first, npro>(emit);
            unsigned xw[3];
            sfor<0, 3>([&](auto M) { xw[M] = lds0 + L_XCH + ((((wid + M + 1) & 3) * 3 + M) * 5) * FRAG + voff; });
            sfor<0, NS * 20>([&](auto I) {
                constexpr int i = I, s = i / 20;
                constexpr bool last = s == NS - 1;
                constexpr int n = last ? i % 5 : (i % 20) / 4, m = last ? (((i % 20) / 5 + 1) & 3) : i % 4;
                if constexpr (i % 20 == 0) {
                    constexpr int wa = ph == 2 ? SP2.wait(i, K_A, s, 3) : SP4.wait(i, K_A, s, 3);
                    constexpr int ww = ph == 2 ? 63 : SP4.wait(i, K_X, X_WP, s * 5 + 4);
                    constexpr int w0 = (ph == 4 && s < 4) ? SP4.before(i) : 63;
                    wait_vm<cmin(cmin(wa, ww), w0)>();
                }
                if constexpr (ph == 4 && i == 48) {
                    wait_vm<SP4.wait(i, K_X, X_HST, 2)>(); bar();
                    if (tid == 0) signal(ws, C_HB + l * 16 + rp);
                }
                if constexpr (ph == 4 && i == 140) {
                    wait_vm<SP4.wait(i, K_X, X_FLAG, 0)>();
                    pin(flagv);
                    if (__builtin_amdgcn_readfirstlane(flagv) < 16) spin_ge(ws, C_HB + l * 16 + (rt >> 1), 16u);
                }
                if constexpr (ph == 2) mma_w<s == 0, w2_reg(s * 5 + n)>(acc[m][n], A[s % AW][m]);
                else mma_w<s == 0, w4_reg(s * 5 + n)>(acc[m][n], A[s % AW][m]);
                if constexpr (ph == 2) sfor<SP2.before(i), SP2.before(i + 1)>(emit); else sfor<SP4.before(i), SP4.before(i + 1)>(emit);
                if constexpr (last && i % 20 >= 10) { constexpr int q = i % 20 - 10, t = q / 5 + 1; ds_wr<(q % 5) * FRAG>(xw[t - 1], acc[t][q % 5]); }
            });
            if constexpr (ph == 2) sfor<0, W4_G>([&](auto J) { ld_w<w4_reg(J), (J % 4) * FRAG>(wP4 + (size_t)(J / 4) * 4 * FRAG, voff); });
            mma_settle();
            sfor<0, 4>([&](auto M) { sfor<0, 5>([&](auto N) { mma_fence(acc[M][N]); }); });
            fresh_lanes();
            sfor<0, 5>([&](auto N) { ds_wr<N * FRAG>(xw[2], acc[3][N]); });
            wait_lgkm<0>(); bar();
            f32x4 fin[5];
            sfor<0, 5>([&](auto N) {
                const unsigned o = L_XCH + (wid * 15 + N) * FRAG + voff;
                fin[N] = (acc[0][N] + *(LAS f32x4*)(lds + o)) + (*(LAS f32x4*)(lds + o + 5 * FRAG) + *(LAS f32x4*)(lds + o + 10 * FRAG));
            });
            auto fin_pair = [&](const f32x4& lo, const f32x4& hi, const u32x4& x) {
                u32x4 w;
                w[0] = pk_bf16(lo[0] + bf_lo(x[0]), lo[1] + bf_hi(x[0])); w[1] = pk_bf16(lo[2] + bf_lo(x[1]), lo[3] + bf_hi(x[1]));
                w[2] = pk_bf16(hi[0] + bf_lo(x[2]), hi[1] + bf_hi(x[2])); w[3] = pk_bf16(hi[2] + bf_lo(x[3]), hi[3] + bf_hi(x[3]));
                return w;
            };
            resA = fin_pair(fin[0], fin[1], resA); resB = fin_pair(fin[2], fin[3], resB);
            resH = (u32x2){pk_bf16(fin[4][0] + bf_lo(resH[0]), fin[4][1] + bf_hi(resH[0])), pk_bf16(fin[4][2] + bf_lo(resH[1]), fin[4][3] + bf_hi(resH[1]))};
            if (ph == 4 && l == 1) {
                char* row = a.out + (size_t)(mtf * 16 + r) * 1280;
                *(GAS u32x4*)(row + tA * 64 + g * 16) = resA;
                *(GAS u32x4*)(row + tA * 64 + 64 + g * 16) = resB;
                *(GAS u32x2*)(row + tH * 64 + odd * 32 + g * 8) = resH;
            } else {
                constexpr int xo = ph == 2 ? 1 : 0;
                const char* xb = ws + WS_X + (size_t)xo * XT_BYTES + (size_t)mtf * KSD * FRAG;
                st_v(xb + tA * FRAG, voff, resA); st_v(xb + (tA + 1) * FRAG, voff, resB); st_v2(xb, hoff_f(), resH);
                float s1 = 0.f, s2 = 0.f, t1 = 0.f, t2 = 0.f;
                auto accum = [&](unsigned u, float& a1, float& a2) {
                    const bf16x2_n v = __builtin_bit_cast(bf16x2_n, u), one = __builtin_bit_cast(bf16x2_n, 0x3f803f80u);
                    a1 = __builtin_amdgcn_fdot2_f32_bf16(v, one, a1, false); a2 = __builtin_amdgcn_fdot2_f32_bf16(v, v, a2, false); };
                sfor<0, 4>([&](auto J) { constexpr int j = J; accum(resA[j], s1, s2); accum(resB[j], t1, t2); });
                accum(resH[0], s1, s2); accum(resH[1], t1, t2);
                s1 = xg_sum(s1 + t1); s2 = xg_sum(s2 + t2);
                if (g == 0) st_v2(ws + WS_ST + (size_t)xo * ST_BYTES + (size_t)mtf * 16 * 64, r * 64 + c8 * 8, (u32x2){__float_as_uint(s1), __float_as_uint(s2)});
                wait_vm<0>(); bar();
                if (tid == 0) signal(ws, (ph == 2 ? C_X1 + l * 16 : C_X2) + (rt >> 1));
                fresh_ptrs();
                if constexpr (ph == 2) { w_chunk(IC<V_P3 + 4>{}); w_chunk(IC<V_P3 + 5>{}); }
                else sfor<0, 6>([&](auto V) { w_chunk(IC<V_END + V>{}); });
            }
        };
        if (wid == 0) spin_ge(ws, C_O + l * 16 + (rt >> 1), 16u);
        bar();
        proj(IC<2>{});
        {
            const char* xsrc = ws + WS_X + XT_BYTES + (size_t)mt_a * KSD * FRAG;
            const char* stsrc = ws + WS_ST + ST_BYTES + (size_t)mt_a * 16 * 64;
            const char* hb = ws + WS_H + (size_t)mt_a * KSF * FRAG;
            if (wid == 0) spin_ge(ws, C_X1 + l * 16 + rp, 16u);
            wait_vm<0>(); bar();
            fresh_lanes(); pins(xsrc); fresh_ptrs();
            f32x4 accA[2][8], accB[2][6];
            u32x4 stp[2]; unsigned flagv;
            const char* hsrc[4];
            sfor<0, 4>([&](auto M) { hsrc[M] = ws + WS_H + (size_t)((rt * 4 + ((wid + M) & 3)) * KSF + wid * 14) * FRAG; });
            auto emit = [&](auto K) {
                constexpr Op o = SP3.op[K];
                if constexpr (o.kind == K_A) ld_a<xa_reg(o.b, o.a), (o.a % 4) * FRAG>(xsrc + (size_t)(o.b * KSD + (o.a / 4) * 4) * FRAG, voff);
                else if constexpr (o.kind == K_W) w_op(IC<o.a>{}, IC<o.b>{});
                else if constexpr (o.a == X_STAT) ld_v<o.b * FRAG>(stp[o.b], stsrc, r * 64 + g * 16);
                else if constexpr (o.a == X_WP) ld_w<w4_reg(o.b), (o.b % 4) * FRAG>(wP4 + (size_t)(o.b / 4) * 4 * FRAG, voff);
                else if constexpr (o.a == X_FLAG) ld_v1(flagv, ws + WS_CTL + (size_t)(C_HA + l * 16 + (rt >> 1)) * 256, 0u);
                else if constexpr (o.a == X_HA) ld_x<(o.b >> 2) * FRAG>(AH[o.b >> 2][o.b & 3], hsrc[o.b & 3], voff);
            };
            sfor<0, SP3.before(0)>(emit);
            float rstd[2], mrs[2];
            constexpr int L = 6, R = 7;
            bf16x8 B[R];
            f32x4 cs[8];
            auto rd = [&](auto I, auto LIM) { constexpr int i = I; if constexpr (i < LIM) ds_rd_at<slot_of(V_P3 + i / 20) + (i % 20) * FRAG>(B[i % R], rdb, rdb_hi); };
            sfor<0, L>([&](auto I) { rd(I, IC<160>{}); });
            wait_lgkm<L - 1>();
            sfor<0, 160>([&](auto I) {
                constexpr int i = I, ks = i / 8, n = i % 8;
                if constexpr (n == 0) wait_vm<SP3.wait(i, K_A, ks, 1)>();
                if constexpr (i == 40) {
                    wait_vm<SP3.wait(i, K_X, X_STAT, 1)>();
                    pin(stp[0]); pin(stp[1]);
                    row_stats(stp[0], rstd[0], mrs[0]); row_stats(stp[1], rstd[1], mrs[1]);
                }
                if constexpr (i == 60) { pin(rstd[0]); pin(rstd[1]); pin(mrs[0]); pin(mrs[1]); }
                if constexpr (i % 20 == 20 - L && i / 20 + 1 >= 2 && i / 20 + 1 < 8) { wait_vm<SP3.wait(i, K_W, V_P3 + i / 20 + 1, 4)>(); bar(); }
                if constexpr (i == 154) sfor<0, 8>([&](auto N) { cs[N] = *(LAS f32x4*)(lds + L_CSG + (N * 16 + 4 * g) * 4); });
                mma<ks == 0, xa_reg(0, ks)>(accA[0][n], B[i % R]);
                rd(IC<i + L>{}, IC<160>{});
                mma<ks == 0, xa_reg(1, ks)>(accA[1][n], B[i % R]);
                sfor<SP3.before(i), (i == 159 ? SP3.find(K_X, X_HST, 0) : SP3.before(i + 1))>(emit);
                if constexpr (i + 1 < 160) wait_lgkm<cmin(L - 1, 158 - i)>();
            });
            mma_settle();
            sfor<0, 8>([&](auto N) { mma_fence(accA[0][N]); mma_fence(accA[1][N]); });
            auto h_tile = [&](const f32x4& ga, const f32x4& ua, const f32x4& cgv, const f32x4& cuv, int m) {
                const f32x4 h = (ga * rstd[m] - cgv * mrs[m]) * (ua * rstd[m] - cuv * mrs[m]);
                return (u32x2){pk_bf16(h[0], h[1]), pk_bf16(h[2], h[3])};
            };
            sfor<0, 2>([&](auto P) {
                sfor<0, 2>([&](auto M) {
                    constexpr int p = P, m = M;
                    const u32x2 lo = h_tile(accA[m][4 * p], accA[m][4 * p + 1], cs[4 * p], cs[4 * p + 1], m), hi = h_tile(accA[m][4 * p + 2], accA[m][4 * p + 3], cs[4 * p + 2], cs[4 * p + 3], m);
                    st_v(hb + (size_t)(m * KSF + h_pos_a(cg, p)) * FRAG, voff, (u32x4){lo[0], lo[1], hi[0], hi[1]});
                });
            });
            fresh_ptrs();
            wait_vm<SP3.wait(160, K_W, V_P3 + 8, 4)>(); wait_lgkm<0>(); bar();
            sfor<160, 160 + L>([&](auto I) { rd(I, IC<280>{}); });
            wait_lgkm<L - 1>();
            sfor<160, 280>([&](auto I) {
                constexpr int i = I, ks = (i - 160) / 6, n = (i - 160) % 6;
                if constexpr (i % 20 == 20 - L && i / 20 + 1 < 14) {
                    constexpr int wv = SP3.wait(i, K_W, V_P3 + i / 20 + 1, 4);
                    wait_vm<wv>(); bar();
                }
                if constexpr (i == HA_SIG) { wait_vm<SP3.wait(i, K_X, X_HST, 3)>(); bar(); if (tid == 0) signal(ws, C_HA + l * 16 + rp); }
                if constexpr (i == HA_CHK) {
                    wait_vm<SP3.wait(i, K_X, X_FLAG, 0)>();
                    pin(flagv);
                    if (__builtin_amdgcn_readfirstlane(flagv) < 16) spin_ge(ws, C_HA + l * 16 + (rt >> 1), 16u);
                    sfor<0, 4>([&](auto M) { pins(hsrc[M]); }); pins_done();
                }
                mma<ks == 0, xa_reg(0, ks)>(accB[0][n], B[i % R]);
                rd(IC<i + L>{}, IC<280>{});
                mma<ks == 0, xa_reg(1, ks)>(accB[1][n], B[i % R]);
                sfor<SP3.before(i), SP3.before(i + 1)>(emit);
                if constexpr (i + 1 < 280) wait_lgkm<cmin(L - 1, 278 - i)>();
            });
            sfor<0, 6>([&](auto N) { cs[N] = *(LAS f32x4*)(lds + L_CSG + ((8 + N) * 16 + 4 * g) * 4); });
            mma_settle();
            sfor<0, 6>([&](auto N) { mma_fence(accB[0][N]); mma_fence(accB[1][N]); });
            fresh_lanes();
            u32x2 t6[2];
            sfor<0, 2>([&](auto M) {
                constexpr int m = M;
                const u32x2 lo = h_tile(accB[m][0], accB[m][1], cs[0], cs[1], m), hi = h_tile(accB[m][2], accB[m][3], cs[2], cs[3], m);
                st_v(hb + (size_t)(m * KSF + h_pos_b(cg)) * FRAG, voff, (u32x4){lo[0], lo[1], hi[0], hi[1]});
                t6[m] = h_tile(accB[m][4], accB[m][5], cs[4], cs[5], m);
            });
            {
                const auto s0 = __builtin_amdgcn_permlane16_swap(t6[0][0], t6[1][0], false, false);
                const auto s1 = __builtin_amdgcn_permlane16_swap(t6[0][1], t6[1][1], false, false);
                const u32x4 w = {s0[0], s1[0], s0[1], s1[1]};
                const char* dst = cg < 12 ? hb + (size_t)h_pos_b(16 + (cg >> 1)) * FRAG : ws + WS_DUMMY;
                const unsigned off = cg < 12 ? (g & 1) * KSF * FRAG + (2 * (cg & 1) + (g >> 1)) * 256 + r * 16 : (blockIdx.x * 4 + wid) * FRAG + voff;
                st_v(dst, off, w);
            }
        }
        proj(IC<4>{});
    }
}

extern "C" void kernel_launch(void* const* d_in, const int* in_sizes, int n_in, void* d_out, int out_size, void* d_ws, size_t ws_size, hipStream_t stream) {
    char* ws = (char*)d_ws;
    (void)hipFuncSetAttribute((const void*)prep_kernel, hipFuncAttributeMaxDynamicSharedMemorySize, PREP_LDS);
    (void)hipFuncSetAttribute((const void*)main_kernel, hipFuncAttributeMaxDynamicSharedMemorySize, LDS_BYTES);
    PrepArgs pa{(const char*)d_in[0], (const char*)d_in[1], (const char*)d_in[2], (const char*)d_in[4], (const char*)d_in[5], (const char*)d_in[6], ws, (const char*)d_out};
    hipLaunchKernelGGL(prep_kernel, dim3(PREP_UNITS), dim3(256), PREP_LDS, stream, pa);
    MainArgs ma{ws, (char*)d_out};
    hipLaunchKernelGGL(main_kernel, dim3(256), dim3(256), LDS_BYTES, stream, ma);
}
```
